```python
import torch

def _softmax(x, axis=-1):
    return torch.softmax(x, dim=axis)
def _gelu(x):
    return torch.nn.functional.gelu(x, approximate="tanh")
def _silu(x):
    return torch.nn.functional.silu(x)
def _sigmoid(x):
    return torch.sigmoid(x)
def _layernorm(x, axis=-1, eps=1e-5):
    m = x.mean(dim=axis, keepdim=True)
    v = x.var(dim=axis, keepdim=True, unbiased=False)
    return (x - m) / torch.sqrt(v + eps)
def _scatter_add(tgt, idx, src, axis=0):
    return tgt.clone().index_add_(axis, idx, src)
def _segment_sum(src, seg, n):
    out = torch.zeros((n,) + src.shape[1:], dtype=src.dtype)
    return out.index_add_(0, seg, src)
def _pad(x, pw):
    flat = tuple(v for pair in reversed(pw) for v in pair)
    return torch.nn.functional.pad(x, flat)

kernel_name = 'sequential_preLN__gqa_block_cached__geglu_ffn__decode'

def setup_inputs():

    _w = (lambda a: (lambda t: t.to(torch.bfloat16) if t.is_floating_point() else t)(torch.as_tensor(a)))
    def _A_setup():
        B, Sq, D = 2, 1, 448
        Hq, Hkv, Sk, Dh = 16, 8, 32768, 256
        assert Hq % Hkv == 0, (Hq, Hkv)
        return {
            "x": _w((lambda *s: torch.randn(*s, dtype=torch.bfloat16) * 0.05)(B, Sq, D)),
            "k_cache": _w((lambda *s: torch.randn(*s, dtype=torch.bfloat16) * 3.2)(B, Hkv, Sk, Dh)),
            "v_cache": _w((lambda *s: torch.randn(*s, dtype=torch.bfloat16) * 0.4)(B, Hkv, Sk, Dh)),
            "wq": _w((lambda *s: torch.randn(*s, dtype=torch.bfloat16) * 3.2)(D, Hq * Dh) * (D ** -0.5)),
            "wo": _w((lambda *s: torch.randn(*s, dtype=torch.bfloat16) * 0.4)(Hq * Dh, D) * ((Hq * Dh) ** -0.5)),
        }
    _A_in = _A_setup()

    def _B_setup():
        B, S, D, F = 2, 1, 448, 1792
        return {
            "x": _w((lambda *s: torch.randn(*s, dtype=torch.bfloat16) * 0.05)(B, S, D)),
            "w_gate": _w((lambda *s: torch.randn(*s, dtype=torch.bfloat16) * 0.05)(D, F)),
            "w_up": _w((lambda *s: torch.randn(*s, dtype=torch.bfloat16) * 0.05)(D, F)),
            "w_down": _w((lambda *s: torch.randn(*s, dtype=torch.bfloat16) * 0.05)(F, D)),
        }
    _B_in = _B_setup()

    return {
        "A_x": _A_in["x"],
        "A_k_cache": _A_in["k_cache"],
        "A_v_cache": _A_in["v_cache"],
        "A_wq": _A_in["wq"],
        "A_wo": _A_in["wo"],
        "B_x": _B_in["x"],
        "B_w_gate": _B_in["w_gate"],
        "B_w_up": _B_in["w_up"],
        "B_w_down": _B_in["w_down"],
    }

def reference(A_x, A_k_cache, A_v_cache, A_wq, A_wo, B_x, B_w_gate, B_w_up, B_w_down):
    def _A_ref(x, k_cache, v_cache, wq, wo):
        B, Sq, D = x.shape
        Hq, Dh = 16, 256
        Hkv = k_cache.shape[1]
        rep = Hq // Hkv
        q = (lambda x, *d: x.permute(*d))((x @ wq).reshape(B, Sq, Hq, Dh), 0, 2, 1, 3)
        k = (lambda x, n, axis: torch.repeat_interleave(x, n, dim=axis))(k_cache, rep, axis=1)
        v = (lambda x, n, axis: torch.repeat_interleave(x, n, dim=axis))(v_cache, rep, axis=1)
        scores = torch.einsum("bhqd,bhkd->bhqk", q, k) * (Dh ** -0.5)
        p = _softmax(scores, axis=-1)
        o = torch.einsum("bhqk,bhkd->bhqd", p, v)
        return (lambda x, *d: x.permute(*d))(o, 0, 2, 1, 3).reshape(B, Sq, Hq * Dh) @ wo

    def _B_ref(x, w_gate, w_up, w_down):
        return (_gelu(x @ w_gate) * (x @ w_up)) @ w_down

    x = A_x
    h = x + _A_ref(_layernorm(x, axis=-1), A_k_cache, A_v_cache, A_wq, A_wo)
    h2 = h + _B_ref(_layernorm(h, axis=-1), B_w_gate, B_w_up, B_w_down)
    return h2
```

```cpp
#include <hip/hip_runtime.h>
#include <hip/hip_bf16.h>
#include <cstdint>

#define ROUND_S 1

typedef unsigned u32x4 __attribute__((ext_vector_type(4)));
typedef float f32x4 __attribute__((ext_vector_type(4)));
typedef short bf16x8 __attribute__((ext_vector_type(8)));
typedef unsigned short u16;

constexpr int B = 2, D = 448, HQ = 16, HKV = 8, SK = 32768, DH = 256, F = 1792;
constexpr int NSPLIT = 32, NWAVE = 4, KEYS_WG = SK / NSPLIT, KEYS_WAVE = KEYS_WG / NWAVE, TILES = KEYS_WAVE / 32;

constexpr size_t WS_Q = 0;
constexpr size_t WS_ML = 65536;
constexpr size_t WS_PO = WS_ML + 65536;
constexpr size_t WS_O = WS_PO + (size_t)B * HQ * NSPLIT * DH * 4;
constexpr size_t WS_H = WS_O + 65536;
constexpr size_t WS_ACT = WS_H + 65536;

__device__ __forceinline__ float bf2f(u16 v) { return __builtin_bit_cast(float, (unsigned)v << 16); }
__device__ __forceinline__ unsigned cvtpk(float lo, float hi) {
  unsigned r; asm("v_cvt_pk_bf16_f32 %0, %1, %2" : "=v"(r) : "v"(lo), "v"(hi)); return r;
}
__device__ __forceinline__ float rbf(float x) { return __builtin_bit_cast(float, cvtpk(x, x) << 16); }
__device__ __forceinline__ f32x4 mfma(u32x4 a, u32x4 b, f32x4 c) {
  return __builtin_amdgcn_mfma_f32_16x16x32_bf16(__builtin_bit_cast(bf16x8, a), __builtin_bit_cast(bf16x8, b), c, 0, 0, 0);
}
template <int CTRL> __device__ __forceinline__ float dpp(float x) {
  return __builtin_bit_cast(float, __builtin_amdgcn_mov_dpp(__builtin_bit_cast(int, x), CTRL, 0xf, 0xf, true));
}
__device__ __forceinline__ float xrow_max(float x) {
  auto s = __builtin_amdgcn_permlane16_swap(__float_as_uint(x), __float_as_uint(x), false, false);
  x = fmaxf(__uint_as_float(s[0]), __uint_as_float(s[1]));
  auto t = __builtin_amdgcn_permlane32_swap(__float_as_uint(x), __float_as_uint(x), false, false);
  return fmaxf(__uint_as_float(t[0]), __uint_as_float(t[1]));
}
__device__ __forceinline__ float xrow_sum(float x) {
  auto s = __builtin_amdgcn_permlane16_swap(__float_as_uint(x), __float_as_uint(x), false, false);
  x = __uint_as_float(s[0]) + __uint_as_float(s[1]);
  auto t = __builtin_amdgcn_permlane32_swap(__float_as_uint(x), __float_as_uint(x), false, false);
  return __uint_as_float(t[0]) + __uint_as_float(t[1]);
}
__device__ __forceinline__ float wave_sum(float v) {
#pragma unroll
  for (int m = 32; m >= 1; m >>= 1) v += __shfl_xor(v, m);
  return v;
}

__device__ __forceinline__ void ln_row(const float (&x)[7], float (&y)[7]) {
  float s = 0.f;
#pragma unroll
  for (int i = 0; i < 7; ++i) s += x[i];
  const float mean = wave_sum(s) * (1.f / D);
  float v = 0.f;
#pragma unroll
  for (int i = 0; i < 7; ++i) { const float d = x[i] - mean; v += d * d; }
  const float var = rbf(wave_sum(v) * (1.f / D));
  const float mb = rbf(mean);
  const float sd = rbf(sqrtf(rbf(var + 1e-5f)));
#pragma unroll
  for (int i = 0; i < 7; ++i) y[i] = rbf(rbf(x[i] - mb) / sd);
}

__global__ void __launch_bounds__(256) k_qproj(const u16* x, const u16* wq, u16* q) {
  __shared__ float xn[B][D];
  __shared__ float red[32][B][64];
  const int tid = threadIdx.x, lane = tid & 63, wave = tid >> 6;
  if (wave < B) {
    float xv[7], yv[7];
#pragma unroll
    for (int i = 0; i < 7; ++i) xv[i] = bf2f(x[wave * D + lane + 64 * i]);
    ln_row(xv, yv);
#pragma unroll
    for (int i = 0; i < 7; ++i) xn[wave][lane + 64 * i] = yv[i];
  }
  const int c8 = tid & 7, rg = tid >> 3;
  u32x4 w[14];
#pragma unroll
  for (int i = 0; i < 14; ++i) w[i] = *reinterpret_cast<const u32x4*>(wq + (size_t)(rg + 32 * i) * 4096 + blockIdx.x * 64 + c8 * 8);
  __syncthreads();
  float acc[B][8] = {};
#pragma unroll
  for (int i = 0; i < 14; ++i) {
    const float x0 = xn[0][rg + 32 * i], x1 = xn[1][rg + 32 * i];
#pragma unroll
    for (int k = 0; k < 4; ++k) {
      const float lo = __builtin_bit_cast(float, w[i][k] << 16), hi = __builtin_bit_cast(float, w[i][k] & 0xffff0000u);
      acc[0][2 * k] = fmaf(x0, lo, acc[0][2 * k]); acc[0][2 * k + 1] = fmaf(x0, hi, acc[0][2 * k + 1]);
      acc[1][2 * k] = fmaf(x1, lo, acc[1][2 * k]); acc[1][2 * k + 1] = fmaf(x1, hi, acc[1][2 * k + 1]);
    }
  }
#pragma unroll
  for (int b = 0; b < B; ++b)
#pragma unroll
    for (int k = 0; k < 8; ++k) red[rg][b][c8 * 8 + k] = acc[b][k];
  __syncthreads();
  if (tid < 128) {
    const int b = tid >> 6, c = tid & 63;
    float s = 0.f;
#pragma unroll
    for (int r = 0; r < 32; ++r) s += red[r][b][c];
    q[b * 4096 + blockIdx.x * 64 + c] = (u16)(cvtpk(s, s) & 0xffffu);
  }
}

__global__ void __launch_bounds__(NWAVE * 64, 2) k_attn(const u16* __restrict__ kc, const u16* __restrict__ vc, const u16* __restrict__ q, float2* pml, float* po) {
  __shared__ float mrg[NWAVE][2][DH + 4];
  const int pair = blockIdx.x % 16, split = blockIdx.x / 16;
  const int b = pair >> 3, hkv = pair & 7;
  const int tid = threadIdx.x, lane = tid & 63, wave = __builtin_amdgcn_readfirstlane(tid >> 6);
  const int g = lane >> 4, col = lane & 15, x4 = (lane >> 2) & 3, cb = lane & 3, head = (lane >> 2) & 1;
  constexpr float C2 = 0.0625f * 1.4426950408889634f;
  u32x4 qf[2];
#pragma unroll
  for (int hf = 0; hf < 2; ++hf) qf[hf] = *reinterpret_cast<const u32x4*>(q + b * 4096 + (hkv * 2 + head) * DH + hf * 128 + (4 * g + cb) * 8);
  const size_t base = ((size_t)pair * SK + (size_t)split * KEYS_WG + (size_t)wave * 32) * DH;
  const u16* kp = kc + base + (size_t)x4 * DH + (4 * g + cb) * 8;
  const u16* vp = vc + base + (size_t)g * DH + col * 8;
  f32x4 acc[2][8];
#pragma unroll
  for (int hf = 0; hf < 2; ++hf)
#pragma unroll
    for (int e = 0; e < 8; ++e) acc[hf][e] = f32x4{0.f, 0.f, 0.f, 0.f};
  float m = -INFINITY, l = 0.f;
  u32x4 K[8][2], V[8][2];
  auto ldk = [&](int t) {
#pragma unroll
    for (int j = 0; j < 8; ++j)
#pragma unroll
      for (int hf = 0; hf < 2; ++hf) K[j][hf] = __builtin_nontemporal_load(reinterpret_cast<const u32x4*>(kp + (size_t)t * (NWAVE * 32 * DH) + j * 4 * DH + hf * 128));
  };
  auto ldv = [&](int t) {
#pragma unroll
    for (int j = 0; j < 8; ++j)
#pragma unroll
      for (int hf = 0; hf < 2; ++hf) V[j][hf] = __builtin_nontemporal_load(reinterpret_cast<const u32x4*>(vp + (size_t)t * (NWAVE * 32 * DH) + j * 4 * DH + hf * 128));
  };
  ldk(0); ldv(0);
  for (int t = 0; t < TILES; ++t) {
    float s[8];
#pragma unroll
    for (int j = 0; j < 8; ++j) {
      f32x4 d = mfma(K[j][0], qf[0], f32x4{0.f, 0.f, 0.f, 0.f});
      d = mfma(K[j][1], qf[1], d);
      s[j] = (dpp<0x00>(d[0]) + dpp<0x55>(d[1])) + (dpp<0xAA>(d[2]) + dpp<0xFF>(d[3]));
    }
    if (t + 1 < TILES) ldk(t + 1);
#pragma unroll
    for (int j = 0; j < 8; ++j) s[j] = (ROUND_S ? rbf(s[j]) : s[j]) * C2;
    float tm = fmaxf(fmaxf(fmaxf(s[0], s[1]), fmaxf(s[2], s[3])), fmaxf(fmaxf(s[4], s[5]), fmaxf(s[6], s[7])));
    if (__any(tm > m)) {
      const float mn = fmaxf(m, xrow_max(tm));
      const float al = __builtin_amdgcn_exp2f(m - mn);
      const float al_h0 = __builtin_bit_cast(float, __builtin_amdgcn_readlane(__builtin_bit_cast(int, al), 0));
      const float al_h1 = __builtin_bit_cast(float, __builtin_amdgcn_readlane(__builtin_bit_cast(int, al), 4));
      const float aa = (g & 1) ? al_h1 : al_h0;
#pragma unroll
      for (int hf = 0; hf < 2; ++hf)
#pragma unroll
        for (int e = 0; e < 8; ++e) acc[hf][e] *= aa;
      l *= al; m = mn;
    }
    float p[8];
#pragma unroll
    for (int j = 0; j < 8; ++j) { p[j] = __builtin_amdgcn_exp2f(s[j] - m); l += p[j]; }
    const u32x4 pf = {cvtpk(p[0], p[1]), cvtpk(p[2], p[3]), cvtpk(p[4], p[5]), cvtpk(p[6], p[7])};
#pragma unroll
    for (int hf = 0; hf < 2; ++hf)
#pragma unroll
      for (int e = 0; e < 8; ++e) {
        u32x4 vf;
#pragma unroll
        for (int i = 0; i < 4; ++i) vf[i] = __builtin_amdgcn_perm(V[2 * i + 1][hf][e >> 1], V[2 * i][hf][e >> 1], (e & 1) ? 0x07060302u : 0x05040100u);
        acc[hf][e] = mfma(pf, vf, acc[hf][e]);
      }
    if (t + 1 < TILES) ldv(t + 1);
  }
  l = xrow_sum(l);
  if (g < 2) {
#pragma unroll
    for (int hf = 0; hf < 2; ++hf)
#pragma unroll
      for (int e = 0; e < 8; ++e) mrg[wave][g][4 + hf * 128 + col * 8 + e] = acc[hf][e][0];
  }
  if (lane == 0 || lane == 4) { mrg[wave][head][0] = m; mrg[wave][head][1] = l; }
  __syncthreads();
  for (int idx = tid; idx < 2 * DH; idx += NWAVE * 64) {
    const int h = idx >> 8, d = idx & 255;
    float M = -INFINITY;
#pragma unroll
    for (int w = 0; w < NWAVE; ++w) M = fmaxf(M, mrg[w][h][0]);
    float L = 0.f, o = 0.f;
#pragma unroll
    for (int w = 0; w < NWAVE; ++w) { const float wt = __builtin_amdgcn_exp2f(mrg[w][h][0] - M); L = fmaf(wt, mrg[w][h][1], L); o = fmaf(wt, mrg[w][h][4 + d], o); }
    const int row = b * HQ + hkv * 2 + h;
    po[((size_t)row * NSPLIT + split) * DH + d] = o;
    if (d == 0) pml[row * NSPLIT + split] = make_float2(M, L);
  }
}

__global__ void __launch_bounds__(256) k_combine(const float2* pml, const float* po, float* o) {
  const int row = blockIdx.x, d = threadIdx.x;
  float M = -INFINITY;
  for (int s = 0; s < NSPLIT; ++s) M = fmaxf(M, pml[row * NSPLIT + s].x);
  float L = 0.f, a = 0.f;
  for (int s = 0; s < NSPLIT; ++s) { const float2 ml = pml[row * NSPLIT + s]; const float wt = __builtin_amdgcn_exp2f(ml.x - M); L = fmaf(wt, ml.y, L); a = fmaf(wt, po[((size_t)row * NSPLIT + s) * DH + d], a); }
  o[row * DH + d] = rbf(a / L);
}

template <int KDIM>
__device__ __forceinline__ void gemv8(const float* act, const u16* w, int ldw, int c0, float (&out)[B][8], float (*red)[B][8]) {
  const int tid = threadIdx.x;
  float acc[B][8] = {};
#pragma unroll
  for (int i = 0; i < KDIM / 256; ++i) {
    const int r = tid + 256 * i;
    const u32x4 wv = *reinterpret_cast<const u32x4*>(w + (size_t)r * ldw + c0);
    const float x0 = act[r], x1 = act[KDIM + r];
#pragma unroll
    for (int k = 0; k < 4; ++k) {
      const float lo = __builtin_bit_cast(float, wv[k] << 16), hi = __builtin_bit_cast(float, wv[k] & 0xffff0000u);
      acc[0][2 * k] = fmaf(x0, lo, acc[0][2 * k]); acc[0][2 * k + 1] = fmaf(x0, hi, acc[0][2 * k + 1]);
      acc[1][2 * k] = fmaf(x1, lo, acc[1][2 * k]); acc[1][2 * k + 1] = fmaf(x1, hi, acc[1][2 * k + 1]);
    }
  }
#pragma unroll
  for (int b = 0; b < B; ++b)
#pragma unroll
    for (int k = 0; k < 8; ++k) acc[b][k] = wave_sum(acc[b][k]);
  if ((tid & 63) == 0) {
#pragma unroll
    for (int b = 0; b < B; ++b)
#pragma unroll
      for (int k = 0; k < 8; ++k) red[tid >> 6][b][k] = acc[b][k];
  }
  __syncthreads();
#pragma unroll
  for (int b = 0; b < B; ++b)
#pragma unroll
    for (int k = 0; k < 8; ++k) out[b][k] = (red[0][b][k] + red[1][b][k]) + (red[2][b][k] + red[3][b][k]);
}

__global__ void __launch_bounds__(256) k_wo(const float* o, const u16* wo, const u16* x, float* h) {
  __shared__ float red[4][B][8];
  float out[B][8];
  gemv8<4096>(o, wo, D, blockIdx.x * 8, out, red);
  if (threadIdx.x == 0) {
#pragma unroll
    for (int b = 0; b < B; ++b)
#pragma unroll
      for (int k = 0; k < 8; ++k) h[b * D + blockIdx.x * 8 + k] = rbf(bf2f(x[b * D + blockIdx.x * 8 + k]) + rbf(out[b][k]));
  }
}

__global__ void __launch_bounds__(256) k_gu(const float* h, const u16* wg, const u16* wu, float* act) {
  __shared__ float hn[B][D];
  __shared__ float red[64][2][B][32];
  const int tid = threadIdx.x, lane = tid & 63, wave = tid >> 6;
  if (wave < B) {
    float xv[7], yv[7];
#pragma unroll
    for (int i = 0; i < 7; ++i) xv[i] = h[wave * D + lane + 64 * i];
    ln_row(xv, yv);
#pragma unroll
    for (int i = 0; i < 7; ++i) hn[wave][lane + 64 * i] = yv[i];
  }
  __syncthreads();
  const int c4 = tid & 3, rg = tid >> 2;
#pragma unroll
  for (int mtx = 0; mtx < 2; ++mtx) {
    const u16* w = mtx ? wu : wg;
    float acc[B][8] = {};
#pragma unroll
    for (int i = 0; i < 7; ++i) {
      const int r = rg + 64 * i;
      const u32x4 wv = *reinterpret_cast<const u32x4*>(w + (size_t)r * F + blockIdx.x * 32 + c4 * 8);
      const float x0 = hn[0][r], x1 = hn[1][r];
#pragma unroll
      for (int k = 0; k < 4; ++k) {
        const float lo = __builtin_bit_cast(float, wv[k] << 16), hi = __builtin_bit_cast(float, wv[k] & 0xffff0000u);
        acc[0][2 * k] = fmaf(x0, lo, acc[0][2 * k]); acc[0][2 * k + 1] = fmaf(x0, hi, acc[0][2 * k + 1]);
        acc[1][2 * k] = fmaf(x1, lo, acc[1][2 * k]); acc[1][2 * k + 1] = fmaf(x1, hi, acc[1][2 * k + 1]);
      }
    }
#pragma unroll
    for (int b = 0; b < B; ++b)
#pragma unroll
      for (int k = 0; k < 8; ++k) red[rg][mtx][b][c4 * 8 + k] = acc[b][k];
  }
  __syncthreads();
  if (tid < 64) {
    const int b = tid >> 5, c = tid & 31;
    float gs = 0.f, us = 0.f;
    for (int r = 0; r < 64; ++r) { gs += red[r][0][b][c]; us += red[r][1][b][c]; }
    gs = rbf(gs); us = rbf(us);
    const float u = 0.7978845608028654f * (gs + 0.044715f * gs * gs * gs);
    const float ge = rbf(0.5f * gs * (1.f + tanhf(u)));
    act[b * F + blockIdx.x * 32 + c] = rbf(ge * us);
  }
}

__global__ void __launch_bounds__(256) k_down(const float* act, const u16* wd, const float* h, u16* out) {
  __shared__ float red[4][B][8];
  float o[B][8];
  gemv8<F>(act, wd, D, blockIdx.x * 8, o, red);
  if (threadIdx.x == 0) {
#pragma unroll
    for (int b = 0; b < B; ++b)
#pragma unroll
      for (int k = 0; k < 8; ++k) { const float r = h[b * D + blockIdx.x * 8 + k] + rbf(o[b][k]); out[b * D + blockIdx.x * 8 + k] = (u16)(cvtpk(r, r) & 0xffffu); }
  }
}

extern "C" void kernel_launch(void* const* d_in, const int* in_sizes, int n_in, void* d_out, int out_size, void* d_ws, size_t ws_size, hipStream_t stream) {
  const u16* x = (const u16*)d_in[0]; const u16* kc = (const u16*)d_in[1]; const u16* vc = (const u16*)d_in[2];
  const u16* wq = (const u16*)d_in[3]; const u16* wo = (const u16*)d_in[4];
  const u16* wg = (const u16*)d_in[6]; const u16* wu = (const u16*)d_in[7]; const u16* wd = (const u16*)d_in[8];
  char* ws = (char*)d_ws;
  k_qproj<<<64, 256, 0, stream>>>(x, wq, (u16*)(ws + WS_Q));
  k_attn<<<16 * NSPLIT, NWAVE * 64, 0, stream>>>(kc, vc, (const u16*)(ws + WS_Q), (float2*)(ws + WS_ML), (float*)(ws + WS_PO));
  k_combine<<<32, 256, 0, stream>>>((const float2*)(ws + WS_ML), (const float*)(ws + WS_PO), (float*)(ws + WS_O));
  k_wo<<<56, 256, 0, stream>>>((const float*)(ws + WS_O), wo, x, (float*)(ws + WS_H));
  k_gu<<<56, 256, 0, stream>>>((const float*)(ws + WS_H), wg, wu, (float*)(ws + WS_ACT));
  k_down<<<56, 256, 0, stream>>>((const float*)(ws + WS_ACT), wd, (const float*)(ws + WS_H), (u16*)d_out);
}
```

```cpp
#include <hip/hip_runtime.h>
#include <hip/hip_bf16.h>
#include <cstdint>

#define START_INFL 2
#define NOPRELOAD 0
#define NOQWAIT 0
#define WQ_NT 1
#define FRAG_NT 1
#define WARM_MODE 0
#define WARM_NAP 0
#define NOCLEAR 0
#define START_NAP 0
#define NAP_Q 24
#define NAP_C 10
#define NAP_D 10
#define NAP_E0 10
#define NAP_E 14
#define STAMPS 0

typedef unsigned u32x4 __attribute__((ext_vector_type(4)));
typedef unsigned u32x2 __attribute__((ext_vector_type(2)));
typedef int i32x4 __attribute__((ext_vector_type(4)));
typedef float f32x4 __attribute__((ext_vector_type(4)));
typedef short s16x8 __attribute__((ext_vector_type(8)));
typedef short s16x4 __attribute__((ext_vector_type(4)));
typedef float f32x2 __attribute__((ext_vector_type(2)));
typedef __bf16 bf16x2 __attribute__((ext_vector_type(2)));
typedef unsigned short u16;
typedef unsigned long long u64;
#define LAS __attribute__((address_space(3)))

constexpr int DM = 448, SK = 32768, DH = 256, FF = 1792;
constexpr int NWG = 256, NSERV = 64, NSPLIT = 12;
constexpr int PAIR_TILES = SK / 16;
constexpr unsigned EMPTY = 0xFFFFFFFFu;
constexpr unsigned DROP = 0x40000000u;
constexpr int SPIN_CAP = 1 << 15;

constexpr unsigned REC = 640, INBOX = 8192;
constexpr unsigned WS_EPOCH = 0, WS_QG = 0x1000, WS_O = 0x12000, WS_H = 0x16000, WS_ACT = 0x18000, WS_PART = 0x20000, WS_BYTES = WS_PART + NSERV * INBOX;
constexpr unsigned WS_DBG = 0x100000;
constexpr unsigned SLOT = 8 * 1056, RING = 4 * SLOT, L_Q = 4 * RING, MRG_ML = 2048;
constexpr unsigned WQ_PIECE = 1088, NPIECE = 19, L_XN = 8 * NPIECE * 1024, XN_ROW = 960, L_RED = L_XN + 2 * XN_ROW, LDS_BYTES = L_RED + 2048;
static_assert(LDS_BYTES <= 163840 && L_Q + 1024 <= LDS_BYTES && 56 * WQ_PIECE <= L_XN, "LDS map");

struct Params {
  const u16 *x, *kc, *vc, *wq, *wo, *wg, *wu, *wd;
  u16* out;
  unsigned char* ws;
};

__device__ __forceinline__ unsigned cvtpk(float lo, float hi) {
  return __builtin_bit_cast(unsigned, __builtin_convertvector(f32x2{lo, hi}, bf16x2));
}
__device__ __forceinline__ float lo16(unsigned w) { return __builtin_bit_cast(float, w << 16); }
__device__ __forceinline__ float hi16(unsigned w) { return __builtin_bit_cast(float, w & 0xffff0000u); }
__device__ __forceinline__ float rbf(float x) { return lo16(cvtpk(x, x)); }
__device__ __forceinline__ unsigned fbits(float x) { return __builtin_bit_cast(unsigned, x); }
__device__ __forceinline__ float bitsf(unsigned x) { return __builtin_bit_cast(float, x); }
__device__ __forceinline__ unsigned nonempty(unsigned w) { return w == EMPTY ? 0x7FC07FC0u : w; }
__device__ __forceinline__ f32x4 mfma32(u32x4 a, u32x4 b, f32x4 c) {
  return __builtin_amdgcn_mfma_f32_16x16x32_bf16(__builtin_bit_cast(s16x8, a), __builtin_bit_cast(s16x8, b), c, 0, 0, 0);
}
__device__ __forceinline__ f32x4 mfma16(u32x2 a, u32x2 b, f32x4 c) {
  return __builtin_amdgcn_mfma_f32_16x16x16bf16_1k(__builtin_bit_cast(s16x4, a), __builtin_bit_cast(s16x4, b), c, 0, 0, 0);
}
template <int CTRL> __device__ __forceinline__ float dpp(float x) {
  return __builtin_bit_cast(float, __builtin_amdgcn_mov_dpp(__builtin_bit_cast(int, x), CTRL, 0xf, 0xf, true));
}
constexpr int XOR1 = 0xB1, XOR2 = 0x4E, XOR7 = 0x141, ROR8 = 0x128;
__device__ __forceinline__ float swap16_max(float x) { auto s = __builtin_amdgcn_permlane16_swap(fbits(x), fbits(x), false, false); return fmaxf(bitsf(s[0]), bitsf(s[1])); }
__device__ __forceinline__ float swap32_max(float x) { auto s = __builtin_amdgcn_permlane32_swap(fbits(x), fbits(x), false, false); return fmaxf(bitsf(s[0]), bitsf(s[1])); }
__device__ __forceinline__ float swap16_sum(float x) { auto s = __builtin_amdgcn_permlane16_swap(fbits(x), fbits(x), false, false); return bitsf(s[0]) + bitsf(s[1]); }
__device__ __forceinline__ float swap32_sum(float x) { auto s = __builtin_amdgcn_permlane32_swap(fbits(x), fbits(x), false, false); return bitsf(s[0]) + bitsf(s[1]); }
__device__ __forceinline__ float half_sum(float v) {
  v += dpp<XOR1>(v); v += dpp<XOR2>(v); v += dpp<XOR7>(v); v += dpp<ROR8>(v);
  return swap16_sum(v);
}
__device__ __forceinline__ i32x4 make_srd(const void* p, unsigned bytes) {
  const u64 a = (u64)p;
  return i32x4{(int)(unsigned)a, (int)((unsigned)(a >> 32) & 0xffffu), (int)bytes, 0x00020000};
}
#define WAIT_VM(n) asm volatile("s_waitcnt vmcnt(" #n ")" ::: "memory")
#define WAIT_LGKM0() asm volatile("s_waitcnt lgkmcnt(0)" ::: "memory")
#define BARRIER() asm volatile("s_waitcnt lgkmcnt(0)\n\ts_barrier" ::: "memory")

__device__ __forceinline__ void dma_tile(i32x4 srd, unsigned voff, unsigned soff, unsigned lds) {
  unsigned keep;
#define PIECE(o) "s_add_u32 m0, m0, 544\n\ts_nop 0\n\tbuffer_load_dwordx4 %1, %2, %3 offen offset:" #o " nt lds\n\t"
  asm volatile("s_waitcnt lgkmcnt(0)\n\ts_mov_b32 %0, m0\n\ts_mov_b32 m0, %4\n\ts_nop 0\n\tbuffer_load_dwordx4 %1, %2, %3 offen nt lds\n\t"
               PIECE(512) PIECE(1024) PIECE(1536) PIECE(2048) PIECE(2560) PIECE(3072) PIECE(3584) "s_mov_b32 m0, %0"
               : "=&s"(keep) : "v"(voff), "s"(srd), "s"(soff), "s"(lds) : "memory", "scc");
#undef PIECE
}
template <bool NT = false> __device__ __forceinline__ void dma_piece(i32x4 srd, unsigned voff, unsigned soff, unsigned lds) {
  unsigned keep;
  if (NT)
    asm volatile("s_mov_b32 %0, m0\n\ts_mov_b32 m0, %4\n\ts_nop 0\n\tbuffer_load_dwordx4 %1, %2, %3 offen nt lds\n\ts_mov_b32 m0, %0"
                 : "=&s"(keep) : "v"(voff), "s"(srd), "s"(soff), "s"(lds) : "memory");
  else
    asm volatile("s_mov_b32 %0, m0\n\ts_mov_b32 m0, %4\n\ts_nop 0\n\tbuffer_load_dwordx4 %1, %2, %3 offen lds\n\ts_mov_b32 m0, %0"
                 : "=&s"(keep) : "v"(voff), "s"(srd), "s"(soff), "s"(lds) : "memory");
}
__device__ __forceinline__ u32x2 tr_read(LAS unsigned char* p) {
  return __builtin_bit_cast(u32x2, __builtin_amdgcn_ds_read_tr16_b64_v4i16((LAS s16x4*)p));
}
__device__ __forceinline__ u32x4 bfrag(LAS unsigned char* p) {
  const u32x2 a = tr_read(p), b = tr_read(p + 128);
  return u32x4{a[0], a[1], b[0], b[1]};
}
__device__ __forceinline__ void layernorm16(const float (&x)[16], bool live, u32x4& y0, u32x4& y1) {
  float s = 0.f;
#pragma unroll
  for (int i = 0; i < 16; ++i) s += x[i];
  const float mean = half_sum(live ? s : 0.f) * (1.f / DM);
  float v = 0.f;
#pragma unroll
  for (int i = 0; i < 16; ++i) { const float d = x[i] - mean; v = fmaf(d, d, v); }
  const float var = rbf(half_sum(live ? v : 0.f) * (1.f / DM));
  const float mb = rbf(mean), rs = 1.f / rbf(sqrtf(rbf(var + 1e-5f)));
  float y[16];
#pragma unroll
  for (int i = 0; i < 16; i += 2) { const unsigned w = cvtpk(x[i] - mb, x[i + 1] - mb); y[i] = lo16(w) * rs; y[i + 1] = hi16(w) * rs; }
  y0 = u32x4{cvtpk(y[0], y[1]), cvtpk(y[2], y[3]), cvtpk(y[4], y[5]), cvtpk(y[6], y[7])};
  y1 = u32x4{cvtpk(y[8], y[9]), cvtpk(y[10], y[11]), cvtpk(y[12], y[13]), cvtpk(y[14], y[15])};
}
__device__ __forceinline__ unsigned umax4(u32x4 v) { return max(max(v[0], v[1]), max(v[2], v[3])); }

#define LD128(off) __builtin_amdgcn_raw_buffer_load_b128(wsr, (off), 0, 16)
#define LD64(off) __builtin_amdgcn_raw_buffer_load_b64(wsr, (off), 0, 16)
#define LD32(off) __builtin_amdgcn_raw_buffer_load_b32(wsr, (off), 0, 16)
#define ST128(v, off) __builtin_amdgcn_raw_buffer_store_b128((v), wsr, (off), 0, 16)
#define ST64(v, off) __builtin_amdgcn_raw_buffer_store_b64((v), wsr, (off), 0, 16)
#define ST32(v, off) __builtin_amdgcn_raw_buffer_store_b32((v), wsr, (off), 0, 16)
#define POLL_BEGIN for (int spin_ = SPIN_CAP;;) { asm volatile("" ::: "memory");
#define POLL_END(pending, nap) if (!__any(pending) || --spin_ <= 0) break; if (nap) __builtin_amdgcn_s_sleep(nap); }
#define STAMP(i)
#define STAMP_OUT()

extern "C" __global__ void __launch_bounds__(512, 2) fused_decode(Params p) {
  __shared__ __attribute__((aligned(16))) unsigned char smem[LDS_BYTES];
  LAS unsigned char* lds = (LAS unsigned char*)smem;
  const unsigned lds0 = (unsigned)(size_t)lds;
  const int bid = blockIdx.x, lane = threadIdx.x & 63, wave = __builtin_amdgcn_readfirstlane(threadIdx.x >> 6);
  const int g = lane >> 4, col = lane & 15;
  const int xcd = bid & 7, idx = bid >> 3;
  const auto wsr = __builtin_amdgcn_make_buffer_rsrc(p.ws, 0, WS_BYTES, 0x00020000);
  constexpr float C2 = 0.0625f * 1.4426950408889634f;
  STAMP(0);

  if (idx >= NSERV / 8) {
    const int j = (idx - NSERV / 8) * 8 + xcd, pair = j / NSPLIT, mem = j % NSPLIT;
    if (wave > 4) return;
    if (wave == 0) {
      const u32x2 ep = LD64(WS_EPOCH);
      const u64 e1 = (((u64)ep[1] << 32) | ep[0]) + 1;
      const unsigned tlo = (unsigned)e1 << 16, thi = (unsigned)(e1 >> 16);
      STAMP(1);
      __builtin_amdgcn_s_sleep(NAP_Q);
      u32x4 qg[4];
      POLL_BEGIN
      unsigned bad = 0;
#pragma unroll
      for (int i = 0; i < 4; ++i) qg[i] = LD128(WS_QG + (pair >> 3) * 32768 + (pair & 7) * 4096 + i * 1024 + lane * 16);
#pragma unroll
      for (int i = 0; i < 4; ++i) bad |= ((qg[i][0] ^ tlo) >> 16) | (qg[i][1] ^ thi) | ((qg[i][2] ^ tlo) >> 16) | (qg[i][3] ^ thi);
      POLL_END(bad != 0 && !NOQWAIT, 0)
#pragma unroll
      for (int i = 0; i < 4; ++i) *(LAS unsigned*)(lds + L_Q + i * 256 + lane * 4) = (qg[i][0] & 0xffffu) | (qg[i][2] << 16);
      BARRIER();
      STAMP(2);
      STAMP_OUT();
      return;
    }
    const i32x4 ksrd = make_srd(p.kc + (size_t)pair * SK * DH, SK * DH * 2), vsrd = make_srd(p.vc + (size_t)pair * SK * DH, SK * DH * 2);
    const unsigned voff = (lane >> 5) * 4096 + (lane & 31) * 16;
    const int sw = wave - 1;
    const unsigned ring = sw * RING;
    const int lo = PAIR_TILES * mem / NSPLIT + sw, hi = PAIR_TILES * (mem + 1) / NSPLIT;
    const int nt = (hi - lo + 3) >> 2;
    auto soff = [&](int t) { return t < nt ? (unsigned)(lo + 4 * t) * 8192u : (unsigned)(SK * DH * 2); };
    auto issue_k = [&](int t) { dma_tile(ksrd, voff, soff(t), lds0 + ring + (t & 1) * 2 * SLOT); };
    auto issue_v = [&](int t) { dma_tile(vsrd, voff, soff(t), lds0 + ring + (t & 1) * 2 * SLOT + SLOT); };
    if (START_NAP) __builtin_amdgcn_s_sleep(START_NAP);
    issue_k(0);
    issue_v(0); WAIT_VM(8); issue_k(1); WAIT_VM(8); issue_v(1);
    BARRIER();
    STAMP(1);
    u32x4 qf[8];
    f32x4 acc[16];
#pragma unroll
    for (int s = 0; s < 8; ++s) qf[s] = *(LAS u32x4*)(lds + L_Q + (col & 1) * 512 + s * 64 + g * 16);
#pragma unroll
    for (int n = 0; n < 16; ++n) acc[n] = f32x4{0.f, 0.f, 0.f, 0.f};
    float m = -INFINITY, m2 = 0.f, l = 0.f;
    const unsigned krd = ring + (col & 7) * 1056 + (col >> 3) * 512 + g * 16;
    const int vrow = 4 * g + (col >> 2);
    const unsigned vrd = ring + SLOT + (vrow & 7) * 1056 + (vrow >> 3) * 512 + (col & 3) * 8;
#pragma unroll 1
    for (int t = 0; t < nt; ++t) {
      WAIT_VM(16);
      if (t != 0) issue_v(t + 1);
      LAS unsigned char* kp = lds + krd + (t & 1) * 2 * SLOT;
      f32x4 d0 = {0.f, 0.f, 0.f, 0.f}, d1 = d0;
#pragma unroll
      for (int s = 0; s < 8; s += 2) {
        d0 = mfma32(*(LAS u32x4*)(kp + s * 64), qf[s], d0);
        d1 = mfma32(*(LAS u32x4*)(kp + s * 64 + 64), qf[s + 1], d1);
      }
      const unsigned w01 = cvtpk(d0[0] + d1[0], d0[1] + d1[1]), w23 = cvtpk(d0[2] + d1[2], d0[3] + d1[3]);
      const float s0 = lo16(w01), s1 = hi16(w01), s2 = lo16(w23), s3 = hi16(w23);
      const float tm = fmaxf(fmaxf(s0, s1), fmaxf(s2, s3));
      if (__any(tm > m)) {
        const float mn = fmaxf(m, swap32_max(swap16_max(tm)));
        const float al = (mn == m) ? 1.f : __builtin_amdgcn_exp2f((m - mn) * C2);
        const float a0 = dpp<0xA0>(al), a1 = dpp<0xF5>(al);
#pragma unroll
        for (int n = 0; n < 16; ++n) { acc[n][0] *= a0; acc[n][1] *= a1; }
        l *= al; m = mn; m2 = (mn == -INFINITY) ? 0.f : mn * C2;
      }
      const float p0 = __builtin_amdgcn_exp2f(fmaf(s0, C2, -m2)), p1 = __builtin_amdgcn_exp2f(fmaf(s1, C2, -m2));
      const float p2 = __builtin_amdgcn_exp2f(fmaf(s2, C2, -m2)), p3 = __builtin_amdgcn_exp2f(fmaf(s3, C2, -m2));
      l += (p0 + p1) + (p2 + p3);
      const u32x2 pf = u32x2{cvtpk(p0, p1), cvtpk(p2, p3)};
      WAIT_VM(16);
      issue_k(t + 2);
      LAS unsigned char* vp = lds + vrd + (t & 1) * 2 * SLOT;
#pragma unroll
      for (int n = 0; n < 16; ++n) acc[n] = mfma16(pf, tr_read(vp + n * 32), acc[n]);
    }
    WAIT_VM(0);
    STAMP(2);
    l = swap32_sum(swap16_sum(l));
    LAS float* rec = (LAS float*)(lds + ring);
    if (lane < 16) {
#pragma unroll
      for (int n = 0; n < 16; ++n) { rec[n * 32 + lane] = acc[n][0]; rec[n * 32 + 16 + lane] = acc[n][1]; }
      if (lane < 2) { rec[MRG_ML / 4 + 2 * lane] = m; rec[MRG_ML / 4 + 2 * lane + 1] = l; }
    }
    BARRIER();
    STAMP(3);
    if (sw < 2) {
      float mw[4], lw[4];
      f32x4 ow[4];
#pragma unroll
      for (int k = 0; k < 4; ++k) {
        LAS float* r = (LAS float*)(lds + k * RING);
        mw[k] = r[MRG_ML / 4 + 2 * sw]; lw[k] = r[MRG_ML / 4 + 2 * sw + 1];
        ow[k] = *(LAS f32x4*)(r + (lane >> 2) * 32 + sw * 16 + (lane & 3) * 4);
      }
      const float M = fmaxf(fmaxf(mw[0], mw[1]), fmaxf(mw[2], mw[3]));
      float L = 0.f;
      f32x4 o = {0.f, 0.f, 0.f, 0.f};
#pragma unroll
      for (int k = 0; k < 4; ++k) { const float a = (mw[k] == M) ? 1.f : __builtin_amdgcn_exp2f((mw[k] - M) * C2); L = fmaf(a, lw[k], L); o += a * ow[k]; }
      const unsigned ro = WS_PART + ((pair & 7) * 8 + (pair >> 3) * 4 + sw * 2 + (lane >> 5)) * INBOX + mem * REC;
      ST128((u32x4{nonempty(fbits(o[0])), nonempty(fbits(o[1])), nonempty(fbits(o[2])), nonempty(fbits(o[3]))}), ro + (lane & 31) * 16);
      ST64((u32x2{nonempty(fbits(M)), nonempty(fbits(L))}), (lane & 31) == 0 ? ro + 512 : DROP);
    }
    STAMP(4);
    STAMP_OUT();
    return;
  }

  const int sidx = xcd * 8 + idx;
  const bool tail = sidx < 56;
  const bool live = (lane & 31) < 28;
  const unsigned oo = WS_O + ((sidx >> 2) & 1) * 8192 + ((sidx >> 3) * 2 + ((sidx >> 1) & 1)) * 512 + (sidx & 1) * 256;
  {
    const i32x4 qsrd = make_srd(p.wq, DM * 4096 * 2);
    const unsigned qv = (lane >> 3) * 8192 + (lane & 7) * 16 + sidx * 128;
#pragma unroll
    for (int s = 0; s < 7; ++s) dma_piece<WQ_NT>(qsrd, qv, (wave * 7 + s) * 8 * 8192, lds0 + (wave * 7 + s) * WQ_PIECE);
    const u32x4 ee = {EMPTY, EMPTY, EMPTY, EMPTY};
    unsigned co = WS_PART + sidx * INBOX + wave * 1024 + lane * 16;
    if (wave == 7 && lane >= 32) co = lane < 48 ? oo + (lane - 32) * 16 : (lane < 50 ? WS_H + sidx * 128 + (lane - 48) * 16 : (lane >= 56 ? WS_ACT + sidx * 128 + (lane - 56) * 16 : DROP));
    if (!NOCLEAR) ST128(ee, co); else ST128(ee, DROP);
  }
  const u32x2 ep = LD64(WS_EPOCH);
  if (wave == 7) {
    const int xo = (lane >> 5) * DM + (live ? (lane & 31) : 0) * 16;
    const u32x4 xa = *(const u32x4*)(p.x + xo), xb = *(const u32x4*)(p.x + xo + 8);
    float xf[16];
#pragma unroll
    for (int k = 0; k < 4; ++k) { xf[2 * k] = lo16(xa[k]); xf[2 * k + 1] = hi16(xa[k]); xf[8 + 2 * k] = lo16(xb[k]); xf[9 + 2 * k] = hi16(xb[k]); }
    u32x4 y0, y1;
    layernorm16(xf, live, y0, y1);
    LAS unsigned char* xn = lds + L_XN + (lane >> 5) * XN_ROW + (lane & 31) * 32;
    if (live) { *(LAS u32x4*)xn = y0; *(LAS u32x4*)(xn + 16) = y1; }
  }
  WAIT_VM(0);
  STAMP(1);
  BARRIER();
  const u64 e1 = (((u64)ep[1] << 32) | ep[0]) + 1;
  const unsigned tlo = (unsigned)e1 << 16, thi = (unsigned)(e1 >> 16);
  if (wave < 4) {
    f32x4 d0 = {0.f, 0.f, 0.f, 0.f}, d1 = d0;
    LAS unsigned char* ap = lds + L_XN + (col & 1) * XN_ROW + g * 16;
    LAS unsigned char* bp = lds + g * WQ_PIECE + (col >> 2) * 128 + wave * 32 + (col & 3) * 8;
#pragma unroll
    for (int s = 0; s < 14; s += 2) {
      const u32x2 b0 = tr_read(bp + s * 4 * WQ_PIECE), b1 = tr_read(bp + s * 4 * WQ_PIECE + 512);
      const u32x2 b2 = tr_read(bp + (s + 1) * 4 * WQ_PIECE), b3 = tr_read(bp + (s + 1) * 4 * WQ_PIECE + 512);
      d0 = mfma32(*(LAS u32x4*)(ap + s * 64), u32x4{b0[0], b0[1], b1[0], b1[1]}, d0);
      d1 = mfma32(*(LAS u32x4*)(ap + s * 64 + 64), u32x4{b2[0], b2[1], b3[0], b3[1]}, d1);
    }
    const float qv = (g & 1) ? d0[1] + d1[1] : d0[0] + d1[0];
    ST64((u32x2{tlo | (cvtpk(qv, qv) & 0xffffu), thi}), lane < 32 ? WS_QG + g * 32768 + (sidx * 64 + wave * 16 + col) * 8 : DROP);
  }
  STAMP(2);
  BARRIER();
  u32x4 wc[8], wd[7], we[4];
  unsigned xw = 0;
  if (tail && !NOPRELOAD) {
    const unsigned st = lds0 + wave * NPIECE * 1024, wl = lane >> 1, wh = lane & 1;
    const i32x4 osrd = make_srd(p.wo, 4096 * DM * 2), gsrd = make_srd(((wave >> 1) & 1) ? p.wu : p.wg, DM * FF * 2), dsrd = make_srd(p.wd, FF * DM * 2);
#pragma unroll
    for (int s = 0; s < 7; ++s) dma_piece<FRAG_NT>(gsrd, wl * (FF * 2) + wh * 16 + sidx * 64 + (wave & 1) * 32, ((wave >> 2) * 224 + s * 32) * (FF * 2), st + (8 + s) * 1024);
#pragma unroll
    for (int s = 0; s < 8; ++s) dma_piece<FRAG_NT>(osrd, (wh * 256 + wl) * (DM * 2) + sidx * 16, (wave * 512 + s * 32) * (DM * 2), st + s * 1024);
#pragma unroll
    for (int s = 0; s < 4; ++s) dma_piece<FRAG_NT>(dsrd, (wh * 128 + wl) * (DM * 2) + sidx * 16, (wave * 256 + s * 32) * (DM * 2), st + (15 + s) * 1024);
    xw = p.x[((lane >> 3) & 1) * DM + sidx * 8 + (lane & 7)];
    WAIT_VM(0);
    LAS unsigned char* rd = lds + wave * NPIECE * 1024 + g * 256 + col * 8;
#pragma unroll
    for (int s = 0; s < 8; ++s) wc[s] = bfrag(rd + s * 1024);
#pragma unroll
    for (int s = 0; s < 7; ++s) wd[s] = bfrag(rd + (8 + s) * 1024);
#pragma unroll
    for (int s = 0; s < 4; ++s) we[s] = bfrag(rd + (15 + s) * 1024);
  }
  STAMP(3);
  const unsigned rb = col < NSPLIT ? WS_PART + sidx * INBOX + col * REC : DROP;
  if (wave == 0) {
    POLL_BEGIN
    const u32x2 f = LD64(WS_QG + lane * 512);
    POLL_END((((f[0] ^ tlo) >> 16) | (f[1] ^ thi)) != 0, 8)
    POLL_BEGIN
    const unsigned f = LD32(rb + 512);
    POLL_END(__builtin_popcountll(__ballot(f == EMPTY)) > 4 * 2, 16)
  }
  BARRIER();
  STAMP(4);
  {
    u32x4 ov;
    u32x2 ml;
    POLL_BEGIN
    ov = LD128(rb + wave * 64 + g * 16);
    ml = LD64(rb + 512);
    POLL_END(max(umax4(ov), max(ml[0], ml[1])) == EMPTY, 0)
    STAMP(5);
    const float ms = col < NSPLIT ? bitsf(ml[0]) : -INFINITY;
    float M = fmaxf(ms, dpp<XOR1>(ms));
    M = fmaxf(M, dpp<XOR2>(M)); M = fmaxf(M, dpp<XOR7>(M)); M = fmaxf(M, dpp<ROR8>(M));
    const float a = (ms == M) ? 1.f : __builtin_amdgcn_exp2f((ms - M) * C2);
    float v[5] = {a * bitsf(ov[0]), a * bitsf(ov[1]), a * bitsf(ov[2]), a * bitsf(ov[3]), a * bitsf(ml[1])};
#pragma unroll
    for (int k = 0; k < 5; ++k) v[k] += dpp<XOR1>(v[k]);
#pragma unroll
    for (int k = 0; k < 5; ++k) v[k] += dpp<XOR2>(v[k]);
#pragma unroll
    for (int k = 0; k < 5; ++k) v[k] += dpp<XOR7>(v[k]);
#pragma unroll
    for (int k = 0; k < 5; ++k) v[k] += dpp<ROR8>(v[k]);
    const float inv = 1.f / v[4];
    ST64((u32x2{nonempty(cvtpk(v[0] * inv, v[1] * inv)), nonempty(cvtpk(v[2] * inv, v[3] * inv))}), col == 0 ? oo + wave * 32 + g * 8 : DROP);
  }
  STAMP(6);
  if (!tail) { STAMP_OUT(); return; }
  LAS float* red = (LAS float*)(lds + L_RED);
  LAS unsigned char* ain = lds + wave * 4096;
  const int arow = (col & 1) * 2 + ((col >> 1) & 1);
  float h = 0.f;
  {
    __builtin_amdgcn_s_sleep(NAP_C);
    u32x4 o0, o1;
    POLL_BEGIN
    o0 = LD128(WS_O + wave * 1024 + lane * 16);
    o1 = LD128(WS_O + 8192 + wave * 1024 + lane * 16);
    POLL_END(max(umax4(o0), umax4(o1)) == EMPTY, 0)
    STAMP(7);
    *(LAS u32x4*)(ain + (lane >> 5) * 576 + (lane & 31) * 16) = o0;
    *(LAS u32x4*)(ain + (2 + (lane >> 5)) * 576 + (lane & 31) * 16) = o1;
    f32x4 d0 = {0.f, 0.f, 0.f, 0.f}, d1 = d0;
#pragma unroll
    for (int s = 0; s < 8; s += 2) {
      d0 = mfma32(*(LAS u32x4*)(ain + arow * 576 + g * 16 + s * 64), wc[s], d0);
      d1 = mfma32(*(LAS u32x4*)(ain + arow * 576 + g * 16 + s * 64 + 64), wc[s + 1], d1);
    }
    d0 += d1;
    const float v = (g & 1) ? (col < 8 ? d0[1] : d0[3]) : (col < 8 ? d0[0] : d0[2]);
    if (lane < 32) red[(g * 8 + (col & 7)) * 16 + wave * 2 + (col >> 3)] = v;
    BARRIER();
    if (wave == 0) {
      const f32x4 r4 = *(LAS f32x4*)(red + col * 16 + g * 4);
      const float a = swap32_sum(swap16_sum((r4[0] + r4[1]) + (r4[2] + r4[3])));
      h = rbf(lo16(xw) + rbf(a));
      ST32(nonempty(cvtpk(h, dpp<XOR1>(h))), (lane < 16 && !(lane & 1)) ? WS_H + sidx * 128 + lane * 2 : DROP);
    } else if (wave == 7 && sidx == 0) {
      ST64((u32x2{(unsigned)e1, (unsigned)(e1 >> 32)}), lane == 0 ? WS_EPOCH : DROP);
    }
    STAMP(8);
  }
  {
    if (wave == 0) {
      __builtin_amdgcn_s_sleep(NAP_D);
      u32x4 ha, hb;
      const unsigned ho = WS_H + (live ? (lane & 31) : 0) * 256 + (lane >> 5) * 16;
      POLL_BEGIN
      ha = LD128(ho); hb = LD128(ho + 128);
      POLL_END(max(umax4(ha), umax4(hb)) == EMPTY, 0)
      STAMP(9);
      float hf[16];
#pragma unroll
      for (int k = 0; k < 4; ++k) { hf[2 * k] = lo16(ha[k]); hf[2 * k + 1] = hi16(ha[k]); hf[8 + 2 * k] = lo16(hb[k]); hf[9 + 2 * k] = hi16(hb[k]); }
      u32x4 y0, y1;
      layernorm16(hf, live, y0, y1);
      LAS unsigned char* xn = lds + L_XN + (lane >> 5) * XN_ROW + (lane & 31) * 32;
      if (live) { *(LAS u32x4*)xn = y0; *(LAS u32x4*)(xn + 16) = y1; }
    }
    BARRIER();
    STAMP(10);
    f32x4 d0 = {0.f, 0.f, 0.f, 0.f}, d1 = d0;
    LAS unsigned char* ap = lds + L_XN + (col & 1) * XN_ROW + (wave >> 2) * 448 + g * 16;
#pragma unroll
    for (int s = 0; s < 7; ++s) {
      if (s & 1) d1 = mfma32(*(LAS u32x4*)(ap + s * 64), wd[s], d1);
      else d0 = mfma32(*(LAS u32x4*)(ap + s * 64), wd[s], d0);
    }
    if (lane < 32) red[256 + wave * 32 + lane] = (g & 1) ? d0[1] + d1[1] : d0[0] + d1[0];
    BARRIER();
    if (wave == 0) {
      const int ro = 256 + (g & 1) * 32 + (lane >> 5) * 16 + col;
      const float gv = rbf(red[ro] + red[ro + 128]), uv = rbf(red[ro + 64] + red[ro + 192]);
      const float z = 0.7978845608028654f * fmaf(0.044715f * gv * gv, gv, gv);
      const float av = rbf(gv / (1.f + __expf(-2.f * z))) * uv;
      ST32(nonempty(cvtpk(av, dpp<XOR1>(av))), !(lane & 1) ? WS_ACT + sidx * 128 + lane * 2 : DROP);
    }
    STAMP(11);
  }
  {
    if (wave < 7) {
      if (wave == 0) __builtin_amdgcn_s_sleep(NAP_E0); else __builtin_amdgcn_s_sleep(NAP_E);
      u32x4 a;
      POLL_BEGIN
      a = LD128(WS_ACT + (wave * 8 + ((lane >> 2) & 7)) * 128 + (lane >> 5) * 64 + (lane & 3) * 16);
      POLL_END(umax4(a) == EMPTY, 0)
      STAMP(12);
      *(LAS u32x4*)(ain + (lane >> 4) * 320 + col * 16) = a;
      f32x4 d0 = {0.f, 0.f, 0.f, 0.f}, d1 = d0;
#pragma unroll
      for (int s = 0; s < 4; s += 2) {
        d0 = mfma32(*(LAS u32x4*)(ain + arow * 320 + g * 16 + s * 64), we[s], d0);
        d1 = mfma32(*(LAS u32x4*)(ain + arow * 320 + g * 16 + s * 64 + 64), we[s + 1], d1);
      }
      d0 += d1;
      const float v = (g & 1) ? (col < 8 ? d0[1] : d0[3]) : (col < 8 ? d0[0] : d0[2]);
      if (lane < 32) red[(g * 8 + (col & 7)) * 16 + wave * 2 + (col >> 3)] = v;
    } else if (lane < 32) {
      red[(lane >> 1) * 16 + 14 + (lane & 1)] = 0.f;
    }
    BARRIER();
    if (wave == 0) {
      const f32x4 r4 = *(LAS f32x4*)(red + col * 16 + g * 4);
      const float f = h + rbf(swap32_sum(swap16_sum((r4[0] + r4[1]) + (r4[2] + r4[3]))));
      if (lane < 16) p.out[(lane >> 3) * DM + sidx * 8 + (lane & 7)] = (u16)cvtpk(f, f);
    }
    STAMP(13);
  }
  STAMP_OUT();
}

extern "C" void kernel_launch(void* const* d_in, const int* in_sizes, int n_in, void* d_out, int out_size, void* d_ws, size_t ws_size,
                              hipStream_t stream) {
  Params p;
  p.x = (const u16*)d_in[0]; p.kc = (const u16*)d_in[1]; p.vc = (const u16*)d_in[2]; p.wq = (const u16*)d_in[3]; p.wo = (const u16*)d_in[4];
  p.wg = (const u16*)d_in[6]; p.wu = (const u16*)d_in[7]; p.wd = (const u16*)d_in[8];
  p.out = (u16*)d_out; p.ws = (unsigned char*)d_ws;
  fused_decode<<<NWG, 512, 0, stream>>>(p);
}
```

```cpp
#include <hip/hip_runtime.h>
#include <hip/hip_bf16.h>
#include <cstdint>

#define NHOLD 2
#define NSTAT 99
#define ORDER 3
#define ARM_LEFT 24
#define NAP_Q 24
#define NAP_Y 12
#define NAP_Z 11
#define STAMPS 0

typedef unsigned u32x4 __attribute__((ext_vector_type(4)));
typedef unsigned u32x2 __attribute__((ext_vector_type(2)));
typedef int i32x4 __attribute__((ext_vector_type(4)));
typedef float f32x4 __attribute__((ext_vector_type(4)));
typedef short s16x8 __attribute__((ext_vector_type(8)));
typedef short s16x4 __attribute__((ext_vector_type(4)));
typedef float f32x2 __attribute__((ext_vector_type(2)));
typedef __bf16 bf16x2 __attribute__((ext_vector_type(2)));
typedef unsigned short u16;
typedef unsigned long long u64;
#define LAS __attribute__((address_space(3)))

constexpr int DM = 448, SK = 32768, DH = 256, FF = 1792;
constexpr int NWG = 256, NSERV = 64, NSPLIT = 12;
constexpr int PAIR_TILES = SK / 16;
constexpr unsigned EMPTY = 0xFFFFFFFFu;
constexpr unsigned DROP = 0x40000000u;
constexpr int SPIN_CAP = 1 << 15;

constexpr unsigned REC = 1152, INBOX = NSPLIT * REC;
constexpr unsigned WS_EPOCH = 0, WS_QG = 0x1000, WS_FLAG = 0x11000, WS_TICKET = 0x11800, WS_Y = 0x12000, WS_Z = 0x20000, WS_PART = 0x60000,
                   WS_BYTES = WS_PART + 32 * INBOX;
constexpr unsigned WS_DBG = 0x100000;
constexpr unsigned SLOT = 8 * 1056, RING = 4 * SLOT, L_Q = 4 * RING, L_REC = L_Q + 1024, RECB = 2304, MRG_ML = 2048;
constexpr unsigned WQ_PIECE = 1088, L_WO = 0, L_WG = 65536, L_WD = L_WG + 2 * 28672, L_XN = L_WD + 28672, XN_ROW = 960, L_ONES = L_XN + 2 * XN_ROW,
                   L_RED = L_ONES + 896, L_STAT = L_RED + 1536, LDS_BYTES = L_STAT + 64;
constexpr unsigned O_ROW = 576, L_O = 0, L_ACT = 2048;
static_assert(LDS_BYTES <= 163840 && L_REC + 4 * RECB <= LDS_BYTES && 56 * WQ_PIECE <= L_XN && WS_Z + 28 * 56 * 128 <= WS_PART, "maps");

struct Params {
  const u16 *x, *kc, *vc, *wq, *wo, *wg, *wu, *wd;
  u16* out;
  unsigned char* ws;
};

__device__ __forceinline__ unsigned cvtpk(float lo, float hi) {
  return __builtin_bit_cast(unsigned, __builtin_convertvector(f32x2{lo, hi}, bf16x2));
}
__device__ __forceinline__ float lo16(unsigned w) { return __builtin_bit_cast(float, w << 16); }
__device__ __forceinline__ float hi16(unsigned w) { return __builtin_bit_cast(float, w & 0xffff0000u); }
__device__ __forceinline__ float rbf(float x) { return lo16(cvtpk(x, x)); }
__device__ __forceinline__ unsigned fbits(float x) { return __builtin_bit_cast(unsigned, x); }
__device__ __forceinline__ float bitsf(unsigned x) { return __builtin_bit_cast(float, x); }
__device__ __forceinline__ unsigned nonempty(unsigned w) { return min(w, EMPTY - 1u); }
__device__ __forceinline__ f32x4 mfma32(u32x4 a, u32x4 b, f32x4 c) {
  return __builtin_amdgcn_mfma_f32_16x16x32_bf16(__builtin_bit_cast(s16x8, a), __builtin_bit_cast(s16x8, b), c, 0, 0, 0);
}
__device__ __forceinline__ f32x4 mfma16(u32x2 a, u32x2 b, f32x4 c) {
  return __builtin_amdgcn_mfma_f32_16x16x16bf16_1k(__builtin_bit_cast(s16x4, a), __builtin_bit_cast(s16x4, b), c, 0, 0, 0);
}
template <int CTRL> __device__ __forceinline__ float dpp(float x) {
  return __builtin_bit_cast(float, __builtin_amdgcn_mov_dpp(__builtin_bit_cast(int, x), CTRL, 0xf, 0xf, true));
}
constexpr int XOR1 = 0xB1, XOR2 = 0x4E, XOR7 = 0x141, ROR8 = 0x128;
__device__ __forceinline__ float swap16_max(float x) { auto s = __builtin_amdgcn_permlane16_swap(fbits(x), fbits(x), false, false); return fmaxf(bitsf(s[0]), bitsf(s[1])); }
__device__ __forceinline__ float swap32_max(float x) { auto s = __builtin_amdgcn_permlane32_swap(fbits(x), fbits(x), false, false); return fmaxf(bitsf(s[0]), bitsf(s[1])); }
__device__ __forceinline__ float swap16_sum(float x) { auto s = __builtin_amdgcn_permlane16_swap(fbits(x), fbits(x), false, false); return bitsf(s[0]) + bitsf(s[1]); }
__device__ __forceinline__ float swap32_sum(float x) { auto s = __builtin_amdgcn_permlane32_swap(fbits(x), fbits(x), false, false); return bitsf(s[0]) + bitsf(s[1]); }
__device__ __forceinline__ float half_sum(float v) {
  v += dpp<XOR1>(v); v += dpp<XOR2>(v); v += dpp<XOR7>(v); v += dpp<ROR8>(v);
  return swap16_sum(v);
}
__device__ __forceinline__ i32x4 make_srd(const void* p, unsigned bytes) {
  const u64 a = (u64)p;
  return i32x4{(int)(unsigned)a, (int)((unsigned)(a >> 32) & 0xffffu), (int)bytes, 0x00020000};
}
#define WAIT_VM(n) asm volatile("s_waitcnt vmcnt(" #n ")" ::: "memory")
#define WAIT_LGKM0() asm volatile("s_waitcnt lgkmcnt(0)" ::: "memory")
#define BARRIER() asm volatile("s_waitcnt lgkmcnt(0)\n\ts_barrier" ::: "memory")

__device__ __forceinline__ void dma_tile(i32x4 srd, unsigned voff, unsigned soff, unsigned lds) {
  unsigned keep;
#define PIECE(o) "s_add_u32 m0, m0, 544\n\ts_nop 0\n\tbuffer_load_dwordx4 %1, %2, %3 offen offset:" #o " nt lds\n\t"
  asm volatile("s_waitcnt lgkmcnt(0)\n\ts_mov_b32 %0, m0\n\ts_mov_b32 m0, %4\n\ts_nop 0\n\tbuffer_load_dwordx4 %1, %2, %3 offen nt lds\n\t"
               PIECE(512) PIECE(1024) PIECE(1536) PIECE(2048) PIECE(2560) PIECE(3072) PIECE(3584) "s_mov_b32 m0, %0"
               : "=&s"(keep) : "v"(voff), "s"(srd), "s"(soff), "s"(lds) : "memory", "scc");
#undef PIECE
}
__device__ __forceinline__ void dma_piece(i32x4 srd, unsigned voff, unsigned soff, unsigned lds) {
  unsigned keep;
  asm volatile("s_mov_b32 %0, m0\n\ts_mov_b32 m0, %4\n\ts_nop 0\n\tbuffer_load_dwordx4 %1, %2, %3 offen nt lds\n\ts_mov_b32 m0, %0"
               : "=&s"(keep) : "v"(voff), "s"(srd), "s"(soff), "s"(lds) : "memory");
}
__device__ __forceinline__ u32x2 tr_read(LAS unsigned char* p) {
  return __builtin_bit_cast(u32x2, __builtin_amdgcn_ds_read_tr16_b64_v4i16((LAS s16x4*)p));
}
__device__ __forceinline__ u32x4 bfrag(LAS unsigned char* p) {
  const u32x2 a = tr_read(p), b = tr_read(p + 128);
  return u32x4{a[0], a[1], b[0], b[1]};
}
__device__ __forceinline__ void layernorm16(const float (&x)[16], bool live, u32x4& y0, u32x4& y1) {
  float s = 0.f;
#pragma unroll
  for (int i = 0; i < 16; ++i) s += x[i];
  const float mean = half_sum(live ? s : 0.f) * (1.f / DM);
  float v = 0.f;
#pragma unroll
  for (int i = 0; i < 16; ++i) { const float d = x[i] - mean; v = fmaf(d, d, v); }
  const float var = rbf(half_sum(live ? v : 0.f) * (1.f / DM));
  const float mb = rbf(mean), rs = 1.f / rbf(sqrtf(rbf(var + 1e-5f)));
  float y[16];
#pragma unroll
  for (int i = 0; i < 16; i += 2) { const unsigned w = cvtpk(x[i] - mb, x[i + 1] - mb); y[i] = lo16(w) * rs; y[i + 1] = hi16(w) * rs; }
  y0 = u32x4{cvtpk(y[0], y[1]), cvtpk(y[2], y[3]), cvtpk(y[4], y[5]), cvtpk(y[6], y[7])};
  y1 = u32x4{cvtpk(y[8], y[9]), cvtpk(y[10], y[11]), cvtpk(y[12], y[13]), cvtpk(y[14], y[15])};
}
__device__ __forceinline__ unsigned umax4(u32x4 v) { return max(max(v[0], v[1]), max(v[2], v[3])); }

#define LD128(off) __builtin_amdgcn_raw_buffer_load_b128(wsr, (off), 0, 16)
#define LD64(off) __builtin_amdgcn_raw_buffer_load_b64(wsr, (off), 0, 16)
#define LD32(off) __builtin_amdgcn_raw_buffer_load_b32(wsr, (off), 0, 16)
#define ST128(v, off) __builtin_amdgcn_raw_buffer_store_b128((v), wsr, (off), 0, 16)
#define ST64(v, off) __builtin_amdgcn_raw_buffer_store_b64((v), wsr, (off), 0, 16)
#define ST32(v, off) __builtin_amdgcn_raw_buffer_store_b32((v), wsr, (off), 0, 16)
#define POLL_BEGIN for (int spin_ = SPIN_CAP;;) { asm volatile("" ::: "memory");
#define POLL_END(pending, nap) if (!__any(pending) || --spin_ <= 0) break; if (nap) __builtin_amdgcn_s_sleep(nap); }
#define STAMP(i)
#define STAMP_OUT()
#define TL(i)
#define TL_OUT()

extern "C" __global__ void __launch_bounds__(512, 2) fused_decode(Params p) {
  __shared__ __attribute__((aligned(16))) unsigned char smem[LDS_BYTES];
  LAS unsigned char* lds = (LAS unsigned char*)smem;
  const unsigned lds0 = (unsigned)(size_t)lds;
  const int bid = blockIdx.x, lane = threadIdx.x & 63, wave = __builtin_amdgcn_readfirstlane(threadIdx.x >> 6);
  const int g = lane >> 4, col = lane & 15;
  const int xcd = bid & 7, idx = bid >> 3;
  const auto wsr = __builtin_amdgcn_make_buffer_rsrc(p.ws, 0, WS_BYTES, 0x00020000);
  constexpr float C2 = 0.0625f * 1.4426950408889634f;
  STAMP(0);

  if (idx >= NSERV / 8) {
    const int j = (idx - NSERV / 8) * 8 + xcd, pair = j / NSPLIT, mem = j % NSPLIT;
    if (wave > 4) return;
    if (wave == 0) {
      const u32x2 ep = LD64(WS_EPOCH);
      const u64 e1 = (((u64)ep[1] << 32) | ep[0]) + 1;
      const unsigned tlo = (unsigned)e1 << 16, thi = (unsigned)(e1 >> 16);
      STAMP(1);
      __builtin_amdgcn_s_sleep(NAP_Q);
      u32x4 qg[4];
      POLL_BEGIN
      unsigned bad = 0;
#pragma unroll
      for (int i = 0; i < 4; ++i) qg[i] = LD128(WS_QG + (pair >> 3) * 32768 + (pair & 7) * 4096 + i * 1024 + lane * 16);
#pragma unroll
      for (int i = 0; i < 4; ++i) bad |= ((qg[i][0] ^ tlo) >> 16) | (qg[i][1] ^ thi) | ((qg[i][2] ^ tlo) >> 16) | (qg[i][3] ^ thi);
      POLL_END(bad != 0, 0)
#pragma unroll
      for (int i = 0; i < 4; ++i) *(LAS unsigned*)(lds + L_Q + i * 256 + lane * 4) = (qg[i][0] & 0xffffu) | (qg[i][2] << 16);
      BARRIER();
      STAMP(2);
      STAMP_OUT();
      return;
    }
    const i32x4 ksrd = make_srd(p.kc + (size_t)pair * SK * DH, SK * DH * 2), vsrd = make_srd(p.vc + (size_t)pair * SK * DH, SK * DH * 2);
    const unsigned voff = (lane >> 5) * 4096 + (lane & 31) * 16;
    const int sw = wave - 1;
    const unsigned ring = sw * RING;
    constexpr unsigned NONE = SK * DH * 2;
    const int wid = mem * 4 + sw;
    auto stat = [&](int t) { return (t * (4 * NSPLIT) + wid) < PAIR_TILES ? (unsigned)(t * (4 * NSPLIT) + wid) * 8192u : NONE; };
    auto issue_k = [&](int t, unsigned so) { dma_tile(ksrd, voff, so, lds0 + ring + (t & 1) * 2 * SLOT); };
    auto issue_v = [&](int t, unsigned so) { dma_tile(vsrd, voff, so, lds0 + ring + (t & 1) * 2 * SLOT + SLOT); };
    const unsigned krd = ring + (col & 7) * 1056 + (col >> 3) * 512 + g * 16;
    const int vrow = 4 * g + (col >> 2);
    const unsigned vrd = ring + SLOT + (vrow & 7) * 1056 + (vrow >> 3) * 512 + (col & 3) * 8;
    u32x4 hk[NHOLD][8];
    u32x2 hv[NHOLD][16];
    STAMP(6);
    issue_k(0, stat(0)); issue_v(0, stat(0)); issue_k(1, stat(1));
#pragma unroll
    for (int t = 0; t < NHOLD; ++t) {
      WAIT_VM(16); TL(2 * t); issue_v(t + 1, stat(t + 1));
#pragma unroll
      for (int s = 0; s < 8; ++s) hk[t][s] = *(LAS u32x4*)(lds + krd + (t & 1) * 2 * SLOT + s * 64);
      WAIT_VM(16); TL(2 * t + 1); issue_k(t + 2, stat(t + 2));
#pragma unroll
      for (int n = 0; n < 16; ++n) hv[t][n] = tr_read(lds + vrd + (t & 1) * 2 * SLOT + n * 32);
    }
    WAIT_VM(16); TL(2 * NHOLD); issue_v(NHOLD + 1, stat(NHOLD + 1));
    BARRIER();
    STAMP(1);
    u32x4 qf[8];
#pragma unroll
    for (int s = 0; s < 8; ++s) qf[s] = *(LAS u32x4*)(lds + L_Q + (col & 1) * 512 + s * 64 + g * 16);
    auto scores = [&](auto kf) {
      f32x4 d0 = {0.f, 0.f, 0.f, 0.f}, d1 = d0;
#pragma unroll
      for (int s = 0; s < 8; s += 2) { d0 = mfma32(kf(s), qf[s], d0); d1 = mfma32(kf(s + 1), qf[s + 1], d1); }
      const unsigned w01 = cvtpk(d0[0] + d1[0], d0[1] + d1[1]), w23 = cvtpk(d0[2] + d1[2], d0[3] + d1[3]);
      return f32x4{lo16(w01), hi16(w01), lo16(w23), hi16(w23)};
    };
    float m, m2, l = 0.f;
    auto probs = [&](f32x4 s) {
      const float p0 = __builtin_amdgcn_exp2f(fmaf(s[0], C2, -m2)), p1 = __builtin_amdgcn_exp2f(fmaf(s[1], C2, -m2));
      const float p2 = __builtin_amdgcn_exp2f(fmaf(s[2], C2, -m2)), p3 = __builtin_amdgcn_exp2f(fmaf(s[3], C2, -m2));
      l += (p0 + p1) + (p2 + p3);
      return u32x2{cvtpk(p0, p1), cvtpk(p2, p3)};
    };
    f32x4 acc[16];
    {
      LAS unsigned char* kp = lds + krd + (NHOLD & 1) * 2 * SLOT;
      const f32x4 sr = scores([&](int s) { return *(LAS u32x4*)(kp + s * 64); });
      issue_k(NHOLD + 2, stat(NHOLD + 2));
      f32x4 sh[NHOLD];
      float tm = fmaxf(fmaxf(sr[0], sr[1]), fmaxf(sr[2], sr[3]));
#pragma unroll
      for (int t = 0; t < NHOLD; ++t) {
        sh[t] = scores([&](int s) { return hk[t][s]; });
        tm = fmaxf(tm, fmaxf(fmaxf(sh[t][0], sh[t][1]), fmaxf(sh[t][2], sh[t][3])));
      }
      m = swap32_max(swap16_max(tm));
      m2 = (m == -INFINITY) ? 0.f : m * C2;
      const u32x2 pr = probs(sr);
      WAIT_VM(24); TL(2 * NHOLD + 1);
      LAS unsigned char* vp = lds + vrd + (NHOLD & 1) * 2 * SLOT;
#pragma unroll
      for (int n = 0; n < 16; ++n) acc[n] = mfma16(pr, tr_read(vp + n * 32), f32x4{0.f, 0.f, 0.f, 0.f});
      issue_v(NHOLD + 2, stat(NHOLD + 2));
#pragma unroll
      for (int t = 0; t < NHOLD; ++t) {
        const u32x2 ph = probs(sh[t]);
#pragma unroll
        for (int n = 0; n < 16; ++n) acc[n] = mfma16(ph, hv[t][n], acc[n]);
      }
    }
    STAMP(5);
    unsigned pub_o = WS_PART + (((pair & 7) * 2 + (pair >> 3)) * 2 + sw) * INBOX + mem * REC + lane * 16;
    unsigned pub_ml = lane == 0 ? pub_o + 1024 : DROP;
    LAS unsigned char* mrd_o = lds + L_REC + (lane >> 2) * 128 + sw * 64 + (lane & 3) * 16;
    LAS unsigned char* mrd_ml = lds + L_REC + MRG_ML + sw * 8;
    asm volatile("" : "+v"(pub_o), "+v"(pub_ml), "+v"(mrd_o), "+v"(mrd_ml));
    const i32x4 tsrd = make_srd(p.ws + WS_TICKET + pair * 128, 4);
    unsigned cur = stat(NHOLD + 1), nxt = stat(NHOLD + 2), tk = 0;
#pragma unroll 1
    for (int t = NHOLD + 1; cur != NONE; ++t) {
      WAIT_VM(16); TL(2 * t);
      if (t != NHOLD + 1) issue_v(t + 1, nxt);
      LAS unsigned char* kp = lds + krd + (t & 1) * 2 * SLOT;
      const f32x4 sc = scores([&](int s) { return *(LAS u32x4*)(kp + s * 64); });
      const float tm = fmaxf(fmaxf(sc[0], sc[1]), fmaxf(sc[2], sc[3]));
      if (__any(tm > m)) {
        const float mn = fmaxf(m, swap32_max(swap16_max(tm)));
        const float al = (mn == m) ? 1.f : __builtin_amdgcn_exp2f((m - mn) * C2);
        const float a0 = dpp<0xA0>(al), a1 = dpp<0xF5>(al);
#pragma unroll
        for (int n = 0; n < 16; ++n) { acc[n][0] *= a0; acc[n][1] *= a1; }
        l *= al; m = mn; m2 = (mn == -INFINITY) ? 0.f : mn * C2;
      }
      const u32x2 pf = probs(sc);
      unsigned got;
      asm volatile("s_waitcnt vmcnt(16)\n\tv_readfirstlane_b32 %0, %1" : "=s"(got) : "v"(tk) : "memory");
      TL(2 * t + 1);
      unsigned nx2 = stat(t + 2);
      if (t + 2 >= NSTAT) nx2 = (nxt != NONE && got + 1u < (unsigned)(PAIR_TILES - NSTAT * 4 * NSPLIT)) ? (NSTAT * 4 * NSPLIT + got + 1u) * 8192u : NONE;
      if (t + 3 >= NSTAT && nx2 != NONE) {
        tk = 1u;
        asm volatile("s_mov_b64 exec, 1\n\ts_nop 4\n\tbuffer_atomic_add %0, off, %1, 0 sc0\n\ts_mov_b64 exec, -1" : "+v"(tk) : "s"(tsrd) : "memory");
      }
      issue_k(t + 2, nx2);
      LAS unsigned char* vp = lds + vrd + (t & 1) * 2 * SLOT;
#pragma unroll
      for (int n = 0; n < 16; ++n) acc[n] = mfma16(pf, tr_read(vp + n * 32), acc[n]);
      cur = nxt; nxt = nx2;
    }
    STAMP(2);
    l = swap32_sum(swap16_sum(l));
    LAS float* rec = (LAS float*)(lds + L_REC + sw * RECB);
    if (lane < 16) {
#pragma unroll
      for (int n = 0; n < 16; ++n) { rec[n * 32 + lane] = acc[n][0]; rec[n * 32 + 16 + lane] = acc[n][1]; }
      if (lane < 2) { rec[MRG_ML / 4 + 2 * lane] = m; rec[MRG_ML / 4 + 2 * lane + 1] = l; }
    }
    BARRIER();
    STAMP(3);
    if (sw < 2) {
      f32x2 ml[4];
      f32x4 ow[4];
#pragma unroll
      for (int k = 0; k < 4; ++k) { ml[k] = *(LAS f32x2*)(mrd_ml + k * RECB); ow[k] = *(LAS f32x4*)(mrd_o + k * RECB); }
      const float M = fmaxf(fmaxf(fmaxf(ml[0][0], ml[1][0]), fmaxf(ml[2][0], ml[3][0])), -3.0e38f);
      float L = 0.f;
      f32x4 o = {0.f, 0.f, 0.f, 0.f};
#pragma unroll
      for (int k = 0; k < 4; ++k) { const float a = __builtin_amdgcn_exp2f((ml[k][0] - M) * C2); L = fmaf(a, ml[k][1], L); o += a * ow[k]; }
      ST128((u32x4{nonempty(fbits(o[0])), nonempty(fbits(o[1])), nonempty(fbits(o[2])), nonempty(fbits(o[3]))}), pub_o);
      ST64((u32x2{nonempty(fbits(M)), nonempty(fbits(L))}), pub_ml);
    } else if (sw == 2) {
      ST32(0u, lane == 0 ? WS_FLAG + pair * 128 + mem * 4 : DROP);
    }
    STAMP(4);
    STAMP_OUT();
    TL_OUT();
    return;
  }

  const int sidx = xcd * 8 + idx, hd = sidx >> 2, cq = sidx & 3;
  const bool ffn = sidx < 56;
  const bool live = (lane & 31) < 28;
  const u32x4 ee = {EMPTY, EMPTY, EMPTY, EMPTY};
  u32x4 xa, xb;
  if (wave == 7) {
    const int xo = (lane >> 5) * DM + (live ? (lane & 31) : 0) * 16;
    xa = *(const u32x4*)(p.x + xo); xb = *(const u32x4*)(p.x + xo + 8);
  } else {
    const i32x4 qsrd = make_srd(p.wq, DM * 4096 * 2);
    const unsigned qv = (lane >> 3) * 8192 + (lane & 7) * 16 + sidx * 128;
#pragma unroll
    for (int s = 0; s < 8; ++s) dma_piece(qsrd, qv, (wave * 8 + s) * 8 * 8192, lds0 + (wave * 8 + s) * WQ_PIECE);
  }
  {
    unsigned co = wave * 1024 + lane * 16 < INBOX / 2 ? WS_PART + sidx * (INBOX / 2) + wave * 1024 + lane * 16 : DROP;
    if (wave == 7 && (sidx & 3) == 0) {
      const int pr = ((sidx >> 2) & 1) * 8 + (sidx >> 3);
      if (lane < 4) co = WS_FLAG + pr * 128 + lane * 16;
      else if (lane == 4) co = WS_TICKET + pr * 128;
    }
    ST128(ee, co);
  }
  const u32x2 ep = LD64(WS_EPOCH);
  if (wave == 7) {
    float xf[16];
#pragma unroll
    for (int k = 0; k < 4; ++k) { xf[2 * k] = lo16(xa[k]); xf[2 * k + 1] = hi16(xa[k]); xf[8 + 2 * k] = lo16(xb[k]); xf[9 + 2 * k] = hi16(xb[k]); }
    u32x4 y0, y1;
    layernorm16(xf, live, y0, y1);
    LAS unsigned char* xn = lds + L_XN + (lane >> 5) * XN_ROW + (lane & 31) * 32;
    if (live) { *(LAS u32x4*)xn = y0; *(LAS u32x4*)(xn + 16) = y1; }
    if (lane < 56) *(LAS u32x4*)(lds + L_ONES + lane * 16) = u32x4{0x3F803F80u, 0x3F803F80u, 0x3F803F80u, 0x3F803F80u};
  }
  WAIT_VM(0);
  STAMP(1);
  BARRIER();
  const u64 e1 = (((u64)ep[1] << 32) | ep[0]) + 1;
  const unsigned tlo = (unsigned)e1 << 16, thi = (unsigned)(e1 >> 16);
  if (wave < 4) {
    f32x4 d0 = {0.f, 0.f, 0.f, 0.f}, d1 = d0;
    LAS unsigned char* ap = lds + L_XN + (col & 1) * XN_ROW + g * 16;
    LAS unsigned char* bp = lds + g * WQ_PIECE + (col >> 2) * 128 + wave * 32 + (col & 3) * 8;
#pragma unroll
    for (int s = 0; s < 14; s += 2) {
      const u32x2 b0 = tr_read(bp + s * 4 * WQ_PIECE), b1 = tr_read(bp + s * 4 * WQ_PIECE + 512);
      const u32x2 b2 = tr_read(bp + (s + 1) * 4 * WQ_PIECE), b3 = tr_read(bp + (s + 1) * 4 * WQ_PIECE + 512);
      d0 = mfma32(*(LAS u32x4*)(ap + s * 64), u32x4{b0[0], b0[1], b1[0], b1[1]}, d0);
      d1 = mfma32(*(LAS u32x4*)(ap + s * 64 + 64), u32x4{b2[0], b2[1], b3[0], b3[1]}, d1);
    }
    const float qv = (g & 1) ? d0[1] + d1[1] : d0[0] + d1[0];
    ST64((u32x2{tlo | (cvtpk(qv, qv) & 0xffffu), thi}), lane < 32 ? WS_QG + g * 32768 + (sidx * 64 + wave * 16 + col) * 8 : DROP);
  }
  STAMP(2);
  BARRIER();
  const int ntile = cq < 3 ? 8 : 4;
  const unsigned yo = WS_Y + hd * 3584 + cq * 512;
  ST128(ee, lane < 2 * ntile * 4 && wave == 0 ? yo + (lane / (ntile * 4)) * 1792 + (lane % (ntile * 4)) * 16 : DROP);
  if (ffn && wave < 4) ST128(ee, wave * 8 + (lane >> 3) < 28 ? WS_Z + ((wave * 8 + (lane >> 3)) * 56 + sidx) * 128 + (lane & 7) * 16 : DROP);
  {
    const i32x4 osrd = make_srd(p.wo, 4096 * DM * 2), gsrd = make_srd(wave < 4 ? p.wg : p.wu, DM * FF * 2), dsrd = make_srd(p.wd, FF * DM * 2);
    const unsigned ov = (lane & 15) < ntile * 2 ? (lane >> 4) * (DM * 2) + (lane & 15) * 16 : DROP;
#pragma unroll
    for (int s = 0; s < 8; ++s) dma_piece(osrd, ov, (hd * 256 + (wave * 8 + s) * 4) * (DM * 2) + cq * 256, lds0 + L_WO + (wave * 8 + s) * 1024);
    if (ffn) {
#pragma unroll
      for (int s = 0; s < 7; ++s) dma_piece(gsrd, (lane >> 2) * (FF * 2) + (lane & 3) * 16, ((wave & 3) * 7 + s) * 16 * (FF * 2) + sidx * 64, lds0 + L_WG + (wave * 7 + s) * 1024);
      if (wave < 7) {
#pragma unroll
        for (int s = 0; s < 4; ++s) dma_piece(dsrd, lane * 16, sidx * 28672 + (wave * 4 + s) * 1024, lds0 + L_WD + (wave * 4 + s) * 1024);
      }
    }
  }
  const int hcol = (wave & 3) * 112 + (live ? (lane & 31) : 0) * 4 + (lane >> 5) * 2;
  const unsigned xw = *(const unsigned*)(p.x + (wave >> 2) * DM + hcol);
  WAIT_VM(0);
  BARRIER();
  if (wave == 0) ST64((u32x2{tlo, thi}), lane == 0 ? WS_FLAG + 64 + (sidx >> 2) * 128 + (sidx & 3) * 8 : DROP);
  u32x4 wc[8], wg[7], wd[4];
  {
    const int q = col >> 2, pp = (col & 3) * 8;
    auto frag = [&](LAS unsigned char* a, int row4) { const u32x2 x = tr_read(a), y = tr_read(a + row4); return u32x4{x[0], x[1], y[0], y[1]}; };
#pragma unroll
    for (int s = 0; s < 8; ++s) wc[s] = frag(lds + L_WO + (32 * s + 8 * g + q) * 256 + wave * 32 + pp, 4 * 256);
#pragma unroll
    for (int s = 0; s < 7; ++s) wg[s] = frag(lds + L_WG + ((wave >> 1) & 1) * 28672 + ((wave >> 2) * 224 + 32 * s + 8 * g + q) * 64 + (wave & 1) * 32 + pp, 4 * 64);
#pragma unroll
    for (int s = 0; s < 4; ++s) wd[s] = frag(lds + L_WD + (8 * g + q) * (DM * 2) + min(wave + 8 * s, 27) * 32 + pp, 4 * DM * 2);
  }
  STAMP(3);
  const unsigned ib = WS_PART + ((hd >> 1) * 4 + (wave >> 2) * 2 + (hd & 1)) * INBOX;
  unsigned mlo = col < NSPLIT ? ib + col * REC + 1024 : DROP, dof = ib + (wave & 3) * 256 + lane * 4;
  unsigned fo = WS_FLAG + (lane >> 2) * 128 + ((lane & 3) < 3 ? (lane & 3) * 16 : 0);
  if (wave == 0) {
    POLL_BEGIN
    const u32x2 f = LD64(WS_FLAG + 64 + (lane >> 2) * 128 + (lane & 3) * 8);
    POLL_END(((f[0] ^ tlo) | (f[1] ^ thi)) != 0, 8)
    POLL_BEGIN
    const u32x4 f = LD128(fo);
    POLL_END(__builtin_popcountll(__ballot(umax4(f) == EMPTY)) > ARM_LEFT, 16)
  }
  LAS unsigned char* o_wr = lds + L_O + (wave >> 2) * O_ROW + (wave & 3) * 128 + lane * 2;
  LAS unsigned char* o_rd = lds + L_O + (col & 1) * O_ROW + g * 16;
  unsigned y_st = (lane < 32 && wave < ntile) ? yo + g * 1792 + wave * 64 + col * 4 : DROP;
  asm volatile("" : "+v"(mlo), "+v"(dof), "+v"(fo), "+v"(o_wr), "+v"(o_rd), "+v"(y_st));
  BARRIER();
  STAMP(4);
  {
    unsigned ov[NSPLIT];
    u32x2 ml;
    bool fdone = wave != 7;
    POLL_BEGIN
    ml = LD64(mlo);
#pragma unroll
    for (int s = 0; s < NSPLIT; ++s) ov[s] = __builtin_amdgcn_raw_buffer_load_b32(wsr, dof, s * REC, 16);
    if (wave == 7) fdone = !__any(umax4(LD128(fo)) == EMPTY);
    unsigned mx = max(ml[0], ml[1]);
#pragma unroll
    for (int s = 0; s < NSPLIT; s += 2) mx = max(mx, max(ov[s], ov[s + 1]));
    POLL_END(mx == EMPTY, 0)
    STAMP(5);
    const float ms = col < NSPLIT ? bitsf(ml[0]) : -INFINITY;
    float M = fmaxf(ms, dpp<XOR1>(ms));
    M = fmaxf(M, dpp<XOR2>(M)); M = fmaxf(M, dpp<XOR7>(M)); M = fmaxf(M, dpp<ROR8>(M));
    const float a = __builtin_amdgcn_exp2f((ms - M) * C2);
    float L = a * bitsf(ml[1]);
    L += dpp<XOR1>(L); L += dpp<XOR2>(L); L += dpp<XOR7>(L); L += dpp<ROR8>(L);
    float o0 = 0.f, o1 = 0.f;
#pragma unroll
    for (int s = 0; s < NSPLIT; s += 2) {
      o0 = fmaf(bitsf(__builtin_amdgcn_readlane(fbits(a), s)), bitsf(ov[s]), o0);
      o1 = fmaf(bitsf(__builtin_amdgcn_readlane(fbits(a), s + 1)), bitsf(ov[s + 1]), o1);
    }
    const float o = (o0 + o1) * __builtin_amdgcn_rcpf(L);
    *(LAS u16*)o_wr = (u16)cvtpk(o, o);
    if (!fdone) {
      POLL_BEGIN
      const u32x4 f = LD128(fo);
      POLL_END(umax4(f) == EMPTY, 0)
    }
    BARRIER();
    STAMP(6);
    u32x4 af[8];
#pragma unroll
    for (int s = 0; s < 8; ++s) af[s] = *(LAS u32x4*)(o_rd + s * 64);
    f32x4 d0 = {0.f, 0.f, 0.f, 0.f}, d1 = d0;
#pragma unroll
    for (int s = 0; s < 8; s += 2) { d0 = mfma32(af[s], wc[s], d0); d1 = mfma32(af[s + 1], wc[s + 1], d1); }
    ST32(nonempty(fbits((g & 1) ? d0[1] + d1[1] : d0[0] + d1[0])), y_st);
  }
  STAMP(7);
  if (!ffn) { STAMP_OUT(); return; }
  LAS float* red = (LAS float*)(lds + L_RED);
  LAS float* stat = (LAS float*)(lds + L_STAT);
  {
    const unsigned yl = live ? WS_Y + (lane >> 5) * 3584 + (wave >> 2) * 1792 + (wave & 3) * 448 + (lane & 31) * 16 : DROP;
    LAS unsigned char* h_wr = lds + L_XN + (wave >> 2) * XN_ROW + hcol * 2;
    LAS unsigned char* h_rd = lds + ((col & 2) ? L_ONES : L_XN + (col & 1) * XN_ROW + (wave >> 2) * 448) + g * 16;
    __builtin_amdgcn_s_sleep(NAP_Y);
    u32x4 y[8];
    POLL_BEGIN
    unsigned mx = 0;
#pragma unroll
    for (int i = 0; i < 8; ++i) y[i] = __builtin_amdgcn_raw_buffer_load_b128(wsr, yl, i * 7168, 16);
#pragma unroll
    for (int i = 0; i < 8; ++i) mx = max(mx, umax4(y[i]));
    POLL_END(mx == EMPTY, 0)
    STAMP(8);
    f32x4 sa = __builtin_bit_cast(f32x4, y[0]) + __builtin_bit_cast(f32x4, y[1]), sb = __builtin_bit_cast(f32x4, y[2]) + __builtin_bit_cast(f32x4, y[3]);
    sa += __builtin_bit_cast(f32x4, y[4]) + __builtin_bit_cast(f32x4, y[5]);
    sb += __builtin_bit_cast(f32x4, y[6]) + __builtin_bit_cast(f32x4, y[7]);
    sa += sb;
    const auto t0 = __builtin_amdgcn_permlane32_swap(fbits(sa[0]), fbits(sa[2]), false, false);
    const auto t1 = __builtin_amdgcn_permlane32_swap(fbits(sa[1]), fbits(sa[3]), false, false);
    const unsigned at = cvtpk(bitsf(t0[0]) + bitsf(t0[1]), bitsf(t1[0]) + bitsf(t1[1]));
    const unsigned hw = cvtpk(lo16(xw) + lo16(at), hi16(xw) + hi16(at));
    if (live) *(LAS unsigned*)h_wr = hw;
    const float h0 = live ? lo16(hw) : 0.f, h1 = live ? hi16(hw) : 0.f;
    const float mw = swap32_sum(half_sum(h0 + h1)) * (1.f / 112);
    const float e0 = live ? h0 - mw : 0.f, e1_ = live ? h1 - mw : 0.f;
    const float qw = swap32_sum(half_sum(fmaf(e0, e0, e1_ * e1_)));
    if (lane == 0) *(LAS f32x2*)(stat + wave * 2) = f32x2{mw, qw};
    BARRIER();
    STAMP(9);
    if (wave == 7 && sidx == 0) ST64((u32x2{(unsigned)e1, (unsigned)(e1 >> 32)}), lane == 0 ? WS_EPOCH : DROP);
    u32x4 af[7];
#pragma unroll
    for (int s = 0; s < 7; ++s) af[s] = *(LAS u32x4*)(h_rd + s * 64);
    f32x4 d0 = {0.f, 0.f, 0.f, 0.f}, d1 = d0;
#pragma unroll
    for (int s = 0; s < 7; ++s) { if (s & 1) d1 = mfma32(af[s], wg[s], d1); else d0 = mfma32(af[s], wg[s], d0); }
    const f32x4 s01 = *(LAS f32x4*)(stat + (lane >> 5) * 8), s23 = *(LAS f32x4*)(stat + (lane >> 5) * 8 + 4);
    const float mean = ((s01[0] + s01[2]) + (s23[0] + s23[2])) * 0.25f;
    const float c0 = s01[0] - mean, c1 = s01[2] - mean, c2 = s23[0] - mean, c3 = s23[2] - mean;
    const float var = rbf((((s01[1] + s01[3]) + (s23[1] + s23[3])) + 112.f * ((c0 * c0 + c1 * c1) + (c2 * c2 + c3 * c3))) * (1.f / DM));
    const float mb = rbf(mean), rs = __builtin_amdgcn_rcpf(rbf(__builtin_amdgcn_sqrtf(rbf(var + 1e-5f))));
    d0 += d1;
    if (lane < 48) red[wave * 48 + lane] = g == 0 ? d0[0] : (g == 1 ? d0[1] : d0[2]);
    BARRIER();
    STAMP(10);
    const int ro = (g & 1) * 48 + col, rb = (lane >> 5) * 16;
    const float gv = rbf(((red[ro + rb] + red[ro + rb + 192]) - mb * (red[ro + 32] + red[ro + 224])) * rs);
    const float uv = rbf(((red[ro + rb + 96] + red[ro + rb + 288]) - mb * (red[ro + 128] + red[ro + 320])) * rs);
    const float z = (-2.f * 0.7978845608028654f * 1.4426950408889634f) * fmaf(0.044715f * gv * gv, gv, gv);
    const float av = rbf(gv * __builtin_amdgcn_rcpf(1.f + __builtin_amdgcn_exp2f(z))) * uv;
    *(LAS u16*)(lds + L_ACT + wave * 128 + lane * 2) = (u16)cvtpk(av, av);
    const u32x4 aa = *(LAS u32x4*)(lds + L_ACT + wave * 128 + (col & 1) * 64 + g * 16);
    f32x4 dz[4];
#pragma unroll
    for (int s = 0; s < 4; ++s) dz[s] = mfma32(aa, wd[s], f32x4{0.f, 0.f, 0.f, 0.f});
#pragma unroll
    for (int s = 0; s < 4; ++s)
      ST32(nonempty(fbits((g & 1) ? dz[s][1] : dz[s][0])), (lane < 32 && wave + 8 * s < 28) ? WS_Z + ((wave + 8 * s) * 56 + sidx) * 128 + lane * 4 : DROP);
  }
  STAMP(11);
  if (sidx < 28 && wave == 7) {
    const unsigned zl = WS_Z + sidx * 7168 + lane * 16;
    const int oc = ((lane & 3) * 4 + (lane >> 5) + ((lane >> 4) & 1) * 2) + sidx * 16, ob = (lane >> 2) & 1;
    const float h = lo16(*(LAS u16*)(lds + L_XN + ob * XN_ROW + oc * 2));
    __builtin_amdgcn_s_sleep(NAP_Z);
    u32x4 z[7];
    POLL_BEGIN
    unsigned mx = 0;
#pragma unroll
    for (int i = 0; i < 7; ++i) z[i] = __builtin_amdgcn_raw_buffer_load_b128(wsr, zl, i * 1024, 16);
#pragma unroll
    for (int i = 0; i < 7; ++i) mx = max(mx, umax4(z[i]));
    POLL_END(mx == EMPTY, 0)
    STAMP(12);
    f32x4 sa = __builtin_bit_cast(f32x4, z[0]) + __builtin_bit_cast(f32x4, z[1]), sb = __builtin_bit_cast(f32x4, z[2]) + __builtin_bit_cast(f32x4, z[3]);
    sa += __builtin_bit_cast(f32x4, z[4]) + __builtin_bit_cast(f32x4, z[5]);
    sa += sb + __builtin_bit_cast(f32x4, z[6]);
    const auto t0 = __builtin_amdgcn_permlane32_swap(fbits(sa[0]), fbits(sa[1]), false, false);
    const auto t1 = __builtin_amdgcn_permlane32_swap(fbits(sa[2]), fbits(sa[3]), false, false);
    const auto t2 = __builtin_amdgcn_permlane16_swap(fbits(bitsf(t0[0]) + bitsf(t0[1])), fbits(bitsf(t1[0]) + bitsf(t1[1])), false, false);
    float f = bitsf(t2[0]) + bitsf(t2[1]);
    f += dpp<ROR8>(f);
    f = h + rbf(f);
    if (!(lane & 8)) p.out[ob * DM + oc] = (u16)cvtpk(f, f);
    STAMP(13);
  }
  STAMP_OUT();
}

extern "C" void kernel_launch(void* const* d_in, const int* in_sizes, int n_in, void* d_out, int out_size, void* d_ws, size_t ws_size,
                              hipStream_t stream) {
  Params p;
  p.x = (const u16*)d_in[0]; p.kc = (const u16*)d_in[1]; p.vc = (const u16*)d_in[2]; p.wq = (const u16*)d_in[3]; p.wo = (const u16*)d_in[4];
  p.wg = (const u16*)d_in[6]; p.wu = (const u16*)d_in[7]; p.wd = (const u16*)d_in[8];
  p.out = (u16*)d_out; p.ws = (unsigned char*)d_ws;
  fused_decode<<<NWG, 512, 0, stream>>>(p);
}
```

```cpp
#include <hip/hip_runtime.h>
#include <hip/hip_bf16.h>
#include <cstdint>

#define KPOL "nt"
#define VPOL "nt"
#define WPOL "nt"
#define TOUCH 0
#define NHOLD 4
#define ORDER 3
#define INFL 2
#define ARM_LEFT 24
#define NAP_Q 24
#define NAP_Y 12
#define NAP_Z 11
#define STAMPS 0

typedef unsigned u32x4 __attribute__((ext_vector_type(4)));
typedef unsigned u32x2 __attribute__((ext_vector_type(2)));
typedef int i32x4 __attribute__((ext_vector_type(4)));
typedef float f32x4 __attribute__((ext_vector_type(4)));
typedef short s16x8 __attribute__((ext_vector_type(8)));
typedef short s16x4 __attribute__((ext_vector_type(4)));
typedef float f32x2 __attribute__((ext_vector_type(2)));
typedef __bf16 bf16x2 __attribute__((ext_vector_type(2)));
typedef unsigned short u16;
typedef unsigned long long u64;
#define LAS __attribute__((address_space(3)))

constexpr int DM = 448, SK = 32768, DH = 256, FF = 1792;
constexpr int NWG = 256, NSERV = 64, NSPLIT = 12;
constexpr int PAIR_TILES = SK / 16;
constexpr unsigned EMPTY = 0xFFFFFFFFu;
constexpr unsigned DROP = 0x40000000u;
constexpr int SPIN_CAP = 1 << 15;

constexpr unsigned REC = 1152, INBOX = NSPLIT * REC;
constexpr unsigned WS_EPOCH = 0, WS_QG = 0x1000, WS_FLAG = 0x11000, WS_Y = 0x12000, WS_Z = 0x20000, WS_PART = 0x60000,
                   WS_BYTES = WS_PART + 32 * INBOX;
constexpr unsigned WS_DBG = 0x100000;
constexpr unsigned SLOT = 8 * 1056, RING = 4 * SLOT, L_Q = 4 * RING, L_REC = L_Q + 1024, RECB = 2304, MRG_ML = 2048, L_SC = L_REC + 4 * RECB, SCB = 2816;
constexpr unsigned WQ_PIECE = 1088, L_WO = 0, L_WG = 65536, L_WD = L_WG + 2 * 28672, L_XN = L_WD + 28672, XN_ROW = 960, L_ONES = L_XN + 2 * XN_ROW,
                   L_RED = L_ONES + 896, L_STAT = L_RED + 1536, LDS_BYTES = L_SC + 4 * SCB;
constexpr unsigned O_ROW = 576, L_O = 0, L_ACT = 2048;
static_assert(LDS_BYTES <= 163840 && L_STAT + 64 <= LDS_BYTES && 56 * WQ_PIECE <= L_XN && WS_Z + 28 * 56 * 128 <= WS_PART, "maps");

struct Params {
  const u16 *x, *kc, *vc, *wq, *wo, *wg, *wu, *wd;
  u16* out;
  unsigned char* ws;
};

__device__ __forceinline__ unsigned cvtpk(float lo, float hi) {
  return __builtin_bit_cast(unsigned, __builtin_convertvector(f32x2{lo, hi}, bf16x2));
}
__device__ __forceinline__ float lo16(unsigned w) { return __builtin_bit_cast(float, w << 16); }
__device__ __forceinline__ float hi16(unsigned w) { return __builtin_bit_cast(float, w & 0xffff0000u); }
__device__ __forceinline__ float rbf(float x) { return lo16(cvtpk(x, x)); }
__device__ __forceinline__ unsigned fbits(float x) { return __builtin_bit_cast(unsigned, x); }
__device__ __forceinline__ float bitsf(unsigned x) { return __builtin_bit_cast(float, x); }
__device__ __forceinline__ unsigned nonempty(unsigned w) { return min(w, EMPTY - 1u); }
__device__ __forceinline__ f32x4 mfma32(u32x4 a, u32x4 b, f32x4 c) {
  return __builtin_amdgcn_mfma_f32_16x16x32_bf16(__builtin_bit_cast(s16x8, a), __builtin_bit_cast(s16x8, b), c, 0, 0, 0);
}
__device__ __forceinline__ f32x4 mfma16(u32x2 a, u32x2 b, f32x4 c) {
  return __builtin_amdgcn_mfma_f32_16x16x16bf16_1k(__builtin_bit_cast(s16x4, a), __builtin_bit_cast(s16x4, b), c, 0, 0, 0);
}
template <int CTRL> __device__ __forceinline__ float dpp(float x) {
  return __builtin_bit_cast(float, __builtin_amdgcn_mov_dpp(__builtin_bit_cast(int, x), CTRL, 0xf, 0xf, true));
}
constexpr int XOR1 = 0xB1, XOR2 = 0x4E, XOR7 = 0x141, ROR8 = 0x128;
__device__ __forceinline__ float swap16_max(float x) { auto s = __builtin_amdgcn_permlane16_swap(fbits(x), fbits(x), false, false); return fmaxf(bitsf(s[0]), bitsf(s[1])); }
__device__ __forceinline__ float swap32_max(float x) { auto s = __builtin_amdgcn_permlane32_swap(fbits(x), fbits(x), false, false); return fmaxf(bitsf(s[0]), bitsf(s[1])); }
__device__ __forceinline__ float swap16_sum(float x) { auto s = __builtin_amdgcn_permlane16_swap(fbits(x), fbits(x), false, false); return bitsf(s[0]) + bitsf(s[1]); }
__device__ __forceinline__ float swap32_sum(float x) { auto s = __builtin_amdgcn_permlane32_swap(fbits(x), fbits(x), false, false); return bitsf(s[0]) + bitsf(s[1]); }
__device__ __forceinline__ float half_sum(float v) {
  v += dpp<XOR1>(v); v += dpp<XOR2>(v); v += dpp<XOR7>(v); v += dpp<ROR8>(v);
  return swap16_sum(v);
}
__device__ __forceinline__ i32x4 make_srd(const void* p, unsigned bytes) {
  const u64 a = (u64)p;
  return i32x4{(int)(unsigned)a, (int)((unsigned)(a >> 32) & 0xffffu), (int)bytes, 0x00020000};
}
#define WAIT_VM(n) asm volatile("s_waitcnt vmcnt(" #n ")" ::: "memory")
#define WAIT_LGKM0() asm volatile("s_waitcnt lgkmcnt(0)" ::: "memory")
#define BARRIER() asm volatile("s_waitcnt lgkmcnt(0)\n\ts_barrier" ::: "memory")

#define DMA_TILE(name, pol)                                                                                                            \
  __device__ __forceinline__ void name(i32x4 srd, unsigned voff, unsigned soff, unsigned lds) {                                        \
    unsigned keep;                                                                                                                     \
    asm volatile("s_waitcnt lgkmcnt(0)\n\ts_mov_b32 %0, m0\n\ts_mov_b32 m0, %4\n\ts_nop 0\n\tbuffer_load_dwordx4 %1, %2, %3 offen " pol " lds\n\t" \
                 PIECE(512, pol) PIECE(1024, pol) PIECE(1536, pol) PIECE(2048, pol) PIECE(2560, pol) PIECE(3072, pol) PIECE(3584, pol)  \
                 "s_mov_b32 m0, %0" : "=&s"(keep) : "v"(voff), "s"(srd), "s"(soff), "s"(lds) : "memory", "scc");                       \
  }
#define PIECE(o, pol) "s_add_u32 m0, m0, 544\n\ts_nop 0\n\tbuffer_load_dwordx4 %1, %2, %3 offen offset:" #o " " pol " lds\n\t"
DMA_TILE(dma_tile_k, KPOL)
DMA_TILE(dma_tile_v, VPOL)
#undef PIECE
__device__ __forceinline__ void dma_piece(i32x4 srd, unsigned voff, unsigned soff, unsigned lds) {
  unsigned keep;
  asm volatile("s_mov_b32 %0, m0\n\ts_mov_b32 m0, %4\n\ts_nop 0\n\tbuffer_load_dwordx4 %1, %2, %3 offen " WPOL " lds\n\ts_mov_b32 m0, %0"
               : "=&s"(keep) : "v"(voff), "s"(srd), "s"(soff), "s"(lds) : "memory");
}
__device__ __forceinline__ u32x2 tr_read(LAS unsigned char* p) {
  return __builtin_bit_cast(u32x2, __builtin_amdgcn_ds_read_tr16_b64_v4i16((LAS s16x4*)p));
}
__device__ __forceinline__ u32x4 bfrag(LAS unsigned char* p) {
  const u32x2 a = tr_read(p), b = tr_read(p + 128);
  return u32x4{a[0], a[1], b[0], b[1]};
}
__device__ __forceinline__ void layernorm16(const float (&x)[16], bool live, u32x4& y0, u32x4& y1) {
  float s = 0.f;
#pragma unroll
  for (int i = 0; i < 16; ++i) s += x[i];
  const float mean = half_sum(live ? s : 0.f) * (1.f / DM);
  float v = 0.f;
#pragma unroll
  for (int i = 0; i < 16; ++i) { const float d = x[i] - mean; v = fmaf(d, d, v); }
  const float var = rbf(half_sum(live ? v : 0.f) * (1.f / DM));
  const float mb = rbf(mean), rs = 1.f / rbf(sqrtf(rbf(var + 1e-5f)));
  float y[16];
#pragma unroll
  for (int i = 0; i < 16; i += 2) { const unsigned w = cvtpk(x[i] - mb, x[i + 1] - mb); y[i] = lo16(w) * rs; y[i + 1] = hi16(w) * rs; }
  y0 = u32x4{cvtpk(y[0], y[1]), cvtpk(y[2], y[3]), cvtpk(y[4], y[5]), cvtpk(y[6], y[7])};
  y1 = u32x4{cvtpk(y[8], y[9]), cvtpk(y[10], y[11]), cvtpk(y[12], y[13]), cvtpk(y[14], y[15])};
}
__device__ __forceinline__ unsigned umax4(u32x4 v) { return max(max(v[0], v[1]), max(v[2], v[3])); }

#define LD128(off) __builtin_amdgcn_raw_buffer_load_b128(wsr, (off), 0, 16)
#define LD64(off) __builtin_amdgcn_raw_buffer_load_b64(wsr, (off), 0, 16)
#define LD32(off) __builtin_amdgcn_raw_buffer_load_b32(wsr, (off), 0, 16)
#define ST128(v, off) __builtin_amdgcn_raw_buffer_store_b128((v), wsr, (off), 0, 16)
#define ST64(v, off) __builtin_amdgcn_raw_buffer_store_b64((v), wsr, (off), 0, 16)
#define ST32(v, off) __builtin_amdgcn_raw_buffer_store_b32((v), wsr, (off), 0, 16)
#define POLL_BEGIN for (int spin_ = SPIN_CAP;;) { asm volatile("" ::: "memory");
#define POLL_END(pending, nap) if (!__any(pending) || --spin_ <= 0) break; if (nap) __builtin_amdgcn_s_sleep(nap); }
#define STAMP(i)
#define STAMP_OUT()
#define TL(i)
#define TL_OUT()

extern "C" __global__ void __launch_bounds__(512, 2) fused_decode(Params p) {
  __shared__ __attribute__((aligned(16))) unsigned char smem[LDS_BYTES];
  LAS unsigned char* lds = (LAS unsigned char*)smem;
  const unsigned lds0 = (unsigned)(size_t)lds;
  const int bid = blockIdx.x, lane = threadIdx.x & 63, wave = __builtin_amdgcn_readfirstlane(threadIdx.x >> 6);
  const int g = lane >> 4, col = lane & 15;
  const int xcd = bid & 7, idx = bid >> 3;
  const auto wsr = __builtin_amdgcn_make_buffer_rsrc(p.ws, 0, WS_BYTES, 0x00020000);
  constexpr float C2 = 0.0625f * 1.4426950408889634f;
  STAMP(0);

  if (idx >= NSERV / 8) {
    const int j = (idx - NSERV / 8) * 8 + xcd, pair = j / NSPLIT, mem = j % NSPLIT;
    if (wave > 4) return;
    if (wave == 0) {
      const u32x2 ep = LD64(WS_EPOCH);
      const u64 e1 = (((u64)ep[1] << 32) | ep[0]) + 1;
      const unsigned tlo = (unsigned)e1 << 16, thi = (unsigned)(e1 >> 16);
      STAMP(1);
      __builtin_amdgcn_s_sleep(NAP_Q);
      u32x4 qg[4];
      POLL_BEGIN
      unsigned bad = 0;
#pragma unroll
      for (int i = 0; i < 4; ++i) qg[i] = LD128(WS_QG + (pair >> 3) * 32768 + (pair & 7) * 4096 + i * 1024 + lane * 16);
#pragma unroll
      for (int i = 0; i < 4; ++i) bad |= ((qg[i][0] ^ tlo) >> 16) | (qg[i][1] ^ thi) | ((qg[i][2] ^ tlo) >> 16) | (qg[i][3] ^ thi);
      POLL_END(bad != 0, 0)
#pragma unroll
      for (int i = 0; i < 4; ++i) *(LAS unsigned*)(lds + L_Q + i * 256 + lane * 4) = (qg[i][0] & 0xffffu) | (qg[i][2] << 16);
      BARRIER();
      STAMP(2);
      STAMP_OUT();
      return;
    }
    const i32x4 ksrd = make_srd(p.kc + (size_t)pair * SK * DH, SK * DH * 2), vsrd = make_srd(p.vc + (size_t)pair * SK * DH, SK * DH * 2);
    const unsigned voff = (lane >> 5) * 4096 + (lane & 31) * 16;
    const int sw = wave - 1;
    const unsigned ring = sw * RING;
    const int wid = mem * 4 + sw;
    const int n = (PAIR_TILES - wid + 4 * NSPLIT - 1) / (4 * NSPLIT);
    const unsigned so0 = wid * 8192u, sstep = 4 * NSPLIT * 8192u;
    auto issue = [&](int u) {
      if (u < n) dma_tile_k(ksrd, voff, so0 + u * sstep, lds0 + ring + (u & 3) * SLOT);
      else dma_tile_v(vsrd, voff, so0 + (u - n) * sstep, lds0 + ring + (u & 3) * SLOT);
    };
    LAS unsigned char* krd = lds + ring + (col & 7) * 1056 + (col >> 3) * 512 + g * 16;
    const int vrow = 4 * g + (col >> 2);
    LAS unsigned char* vrd = lds + ring + (vrow & 7) * 1056 + (vrow >> 3) * 512 + (col & 3) * 8;
    LAS unsigned char* scp = lds + L_SC + sw * SCB + (col & 1) * 32 + g * 8;
    u32x4 hk[NHOLD][8];
    issue(0); issue(1);
#pragma unroll
    for (int u = 0; u < NHOLD; ++u) {
      WAIT_VM(8); TL(u); issue(u + 2);
#pragma unroll
      for (int s = 0; s < 8; ++s) hk[u][s] = *(LAS u32x4*)(krd + (u & 3) * SLOT + s * 64);
    }
    WAIT_VM(8); TL(NHOLD); issue(NHOLD + 2);
    WAIT_VM(8); TL(NHOLD + 1); issue(NHOLD + 3);
    BARRIER();
    STAMP(1);
    u32x4 qf[8];
#pragma unroll
    for (int s = 0; s < 8; ++s) qf[s] = *(LAS u32x4*)(lds + L_Q + (col & 1) * 512 + s * 64 + g * 16);
    float m = -INFINITY;
    auto scores = [&](int u, auto kf) {
      f32x4 d0 = {0.f, 0.f, 0.f, 0.f}, d1 = d0;
#pragma unroll
      for (int s = 0; s < 8; s += 2) { d0 = mfma32(kf(s), qf[s], d0); d1 = mfma32(kf(s + 1), qf[s + 1], d1); }
      const unsigned w01 = cvtpk(d0[0] + d1[0], d0[1] + d1[1]), w23 = cvtpk(d0[2] + d1[2], d0[3] + d1[3]);
      m = fmaxf(fmaxf(m, fmaxf(lo16(w01), hi16(w01))), fmaxf(lo16(w23), hi16(w23)));
      if (col < 2) *(LAS u32x2*)(scp + u * 64) = u32x2{w01, w23};
    };
    auto ring_scores = [&](int u) { scores(u, [&](int s) { return *(LAS u32x4*)(krd + (u & 3) * SLOT + s * 64); }); };
    ring_scores(NHOLD); issue(NHOLD + 4);
    ring_scores(NHOLD + 1); issue(NHOLD + 5);
#pragma unroll
    for (int u = 0; u < NHOLD; ++u) scores(u, [&](int s) { return hk[u][s]; });
    WAIT_VM(24); TL(NHOLD + 2); ring_scores(NHOLD + 2);
    WAIT_VM(16); TL(NHOLD + 3); ring_scores(NHOLD + 3);
    constexpr int U0 = NHOLD + 4;
#define WAIT_STEP() WAIT_VM(8)
    STAMP(5);
#pragma unroll 1
    for (int u = U0; u < n; ++u) { WAIT_STEP(); TL(u); issue(u + INFL); ring_scores(u); }
    m = swap32_max(swap16_max(m));
    const float m2 = fmaxf(m, -3.0e38f) * C2;
    unsigned pub_o = WS_PART + (((pair & 7) * 2 + (pair >> 3)) * 2 + sw) * INBOX + mem * REC + lane * 16;
    unsigned pub_ml = lane == 0 ? pub_o + 1024 : DROP;
    LAS unsigned char* mrd_o = lds + L_REC + (lane >> 2) * 128 + sw * 64 + (lane & 3) * 16;
    LAS unsigned char* mrd_ml = lds + L_REC + MRG_ML + sw * 8;
    asm volatile("" : "+v"(pub_o), "+v"(pub_ml), "+v"(mrd_o), "+v"(mrd_ml));
    f32x4 acc[16];
#pragma unroll
    for (int k = 0; k < 16; ++k) acc[k] = f32x4{0.f, 0.f, 0.f, 0.f};
    float l = 0.f;
#pragma unroll 1
    for (int v = 0; v < n; ++v) {
      const u32x2 sw2 = *(LAS u32x2*)(scp + v * 64);
      const float p0 = __builtin_amdgcn_exp2f(fmaf(lo16(sw2[0]), C2, -m2)), p1 = __builtin_amdgcn_exp2f(fmaf(hi16(sw2[0]), C2, -m2));
      const float p2 = __builtin_amdgcn_exp2f(fmaf(lo16(sw2[1]), C2, -m2)), p3 = __builtin_amdgcn_exp2f(fmaf(hi16(sw2[1]), C2, -m2));
      l += (p0 + p1) + (p2 + p3);
      u32x2 pf = u32x2{cvtpk(p0, p1), cvtpk(p2, p3)};
      asm volatile("" : "+v"(pf), "+v"(l));
      if (v + INFL < n) { WAIT_STEP(); issue(n + v + INFL); }
      else if (v + 2 < n) WAIT_VM(16);
      else if (v + 1 < n) WAIT_VM(8);
      else WAIT_VM(0);
      TL(n + v);
      LAS unsigned char* vp = vrd + ((n + v) & 3) * SLOT;
#pragma unroll
      for (int k = 0; k < 16; ++k) acc[k] = mfma16(pf, tr_read(vp + k * 32), acc[k]);
    }
    STAMP(2);
    l = swap32_sum(swap16_sum(l));
    LAS float* rec = (LAS float*)(lds + L_REC + sw * RECB);
    if (lane < 16) {
#pragma unroll
      for (int k = 0; k < 16; ++k) { rec[k * 32 + lane] = acc[k][0]; rec[k * 32 + 16 + lane] = acc[k][1]; }
      if (lane < 2) { rec[MRG_ML / 4 + 2 * lane] = m; rec[MRG_ML / 4 + 2 * lane + 1] = l; }
    }
    BARRIER();
    STAMP(3);
    if (sw < 2) {
      f32x2 ml[4];
      f32x4 ow[4];
#pragma unroll
      for (int k = 0; k < 4; ++k) { ml[k] = *(LAS f32x2*)(mrd_ml + k * RECB); ow[k] = *(LAS f32x4*)(mrd_o + k * RECB); }
      const float M = fmaxf(fmaxf(fmaxf(ml[0][0], ml[1][0]), fmaxf(ml[2][0], ml[3][0])), -3.0e38f);
      float L = 0.f;
      f32x4 o = {0.f, 0.f, 0.f, 0.f};
#pragma unroll
      for (int k = 0; k < 4; ++k) { const float a = __builtin_amdgcn_exp2f((ml[k][0] - M) * C2); L = fmaf(a, ml[k][1], L); o += a * ow[k]; }
      ST128((u32x4{nonempty(fbits(o[0])), nonempty(fbits(o[1])), nonempty(fbits(o[2])), nonempty(fbits(o[3]))}), pub_o);
      ST64((u32x2{nonempty(fbits(M)), nonempty(fbits(L))}), pub_ml);
    } else if (sw == 2) {
      ST32(0u, lane == 0 ? WS_FLAG + pair * 128 + mem * 4 : DROP);
    }
    STAMP(4);
    STAMP_OUT();
    TL_OUT();
    return;
  }

  const int sidx = xcd * 8 + idx, hd = sidx >> 2, cq = sidx & 3;
  const bool ffn = sidx < 56;
  const bool live = (lane & 31) < 28;
  const u32x4 ee = {EMPTY, EMPTY, EMPTY, EMPTY};
  u32x4 xa, xb;
  if (wave == 7) {
    const int xo = (lane >> 5) * DM + (live ? (lane & 31) : 0) * 16;
    xa = *(const u32x4*)(p.x + xo); xb = *(const u32x4*)(p.x + xo + 8);
  } else {
    const i32x4 qsrd = make_srd(p.wq, DM * 4096 * 2);
    const unsigned qv = (lane >> 3) * 8192 + (lane & 7) * 16 + sidx * 128;
#pragma unroll
    for (int s = 0; s < 8; ++s) dma_piece(qsrd, qv, (wave * 8 + s) * 8 * 8192, lds0 + (wave * 8 + s) * WQ_PIECE);
  }
  {
    unsigned co = wave * 1024 + lane * 16 < INBOX / 2 ? WS_PART + sidx * (INBOX / 2) + wave * 1024 + lane * 16 : DROP;
    if (wave == 7 && (sidx & 3) == 0) {
      const int pr = ((sidx >> 2) & 1) * 8 + (sidx >> 3);
      if (lane < 4) co = WS_FLAG + pr * 128 + lane * 16;
    }
    ST128(ee, co);
  }
  const u32x2 ep = LD64(WS_EPOCH);
  if (wave == 7) {
    float xf[16];
#pragma unroll
    for (int k = 0; k < 4; ++k) { xf[2 * k] = lo16(xa[k]); xf[2 * k + 1] = hi16(xa[k]); xf[8 + 2 * k] = lo16(xb[k]); xf[9 + 2 * k] = hi16(xb[k]); }
    u32x4 y0, y1;
    layernorm16(xf, live, y0, y1);
    LAS unsigned char* xn = lds + L_XN + (lane >> 5) * XN_ROW + (lane & 31) * 32;
    if (live) { *(LAS u32x4*)xn = y0; *(LAS u32x4*)(xn + 16) = y1; }
    if (lane < 56) *(LAS u32x4*)(lds + L_ONES + lane * 16) = u32x4{0x3F803F80u, 0x3F803F80u, 0x3F803F80u, 0x3F803F80u};
  }
  WAIT_VM(0);
  STAMP(1);
  BARRIER();
  const u64 e1 = (((u64)ep[1] << 32) | ep[0]) + 1;
  const unsigned tlo = (unsigned)e1 << 16, thi = (unsigned)(e1 >> 16);
  if (wave < 4) {
    f32x4 d0 = {0.f, 0.f, 0.f, 0.f}, d1 = d0;
    LAS unsigned char* ap = lds + L_XN + (col & 1) * XN_ROW + g * 16;
    LAS unsigned char* bp = lds + g * WQ_PIECE + (col >> 2) * 128 + wave * 32 + (col & 3) * 8;
#pragma unroll
    for (int s = 0; s < 14; s += 2) {
      const u32x2 b0 = tr_read(bp + s * 4 * WQ_PIECE), b1 = tr_read(bp + s * 4 * WQ_PIECE + 512);
      const u32x2 b2 = tr_read(bp + (s + 1) * 4 * WQ_PIECE), b3 = tr_read(bp + (s + 1) * 4 * WQ_PIECE + 512);
      d0 = mfma32(*(LAS u32x4*)(ap + s * 64), u32x4{b0[0], b0[1], b1[0], b1[1]}, d0);
      d1 = mfma32(*(LAS u32x4*)(ap + s * 64 + 64), u32x4{b2[0], b2[1], b3[0], b3[1]}, d1);
    }
    const float qv = (g & 1) ? d0[1] + d1[1] : d0[0] + d1[0];
    ST64((u32x2{tlo | (cvtpk(qv, qv) & 0xffffu), thi}), lane < 32 ? WS_QG + g * 32768 + (sidx * 64 + wave * 16 + col) * 8 : DROP);
  }
  STAMP(2);
  BARRIER();
  const int ntile = cq < 3 ? 8 : 4;
  const unsigned yo = WS_Y + hd * 3584 + cq * 512;
  ST128(ee, lane < 2 * ntile * 4 && wave == 0 ? yo + (lane / (ntile * 4)) * 1792 + (lane % (ntile * 4)) * 16 : DROP);
  if (ffn && wave < 4) ST128(ee, wave * 8 + (lane >> 3) < 28 ? WS_Z + ((wave * 8 + (lane >> 3)) * 56 + sidx) * 128 + (lane & 7) * 16 : DROP);
  {
    const i32x4 osrd = make_srd(p.wo, 4096 * DM * 2), gsrd = make_srd(wave < 4 ? p.wg : p.wu, DM * FF * 2), dsrd = make_srd(p.wd, FF * DM * 2);
    const unsigned ov = (lane & 15) < ntile * 2 ? (lane >> 4) * (DM * 2) + (lane & 15) * 16 : DROP;
#pragma unroll
    for (int s = 0; s < 8; ++s) dma_piece(osrd, ov, (hd * 256 + (wave * 8 + s) * 4) * (DM * 2) + cq * 256, lds0 + L_WO + (wave * 8 + s) * 1024);
    if (ffn) {
#pragma unroll
      for (int s = 0; s < 7; ++s) dma_piece(gsrd, (lane >> 2) * (FF * 2) + (lane & 3) * 16, ((wave & 3) * 7 + s) * 16 * (FF * 2) + sidx * 64, lds0 + L_WG + (wave * 7 + s) * 1024);
      if (wave < 7) {
#pragma unroll
        for (int s = 0; s < 4; ++s) dma_piece(dsrd, lane * 16, sidx * 28672 + (wave * 4 + s) * 1024, lds0 + L_WD + (wave * 4 + s) * 1024);
      }
    }
  }
  const int hcol = (wave & 3) * 112 + (live ? (lane & 31) : 0) * 4 + (lane >> 5) * 2;
  const unsigned xw = *(const unsigned*)(p.x + (wave >> 2) * DM + hcol);
  WAIT_VM(0);
  BARRIER();
  if (wave == 0) ST64((u32x2{tlo, thi}), lane == 0 ? WS_FLAG + 64 + (sidx >> 2) * 128 + (sidx & 3) * 8 : DROP);
  u32x4 wc[8], wg[7], wd[4];
  {
    const int q = col >> 2, pp = (col & 3) * 8;
    auto frag = [&](LAS unsigned char* a, int row4) { const u32x2 x = tr_read(a), y = tr_read(a + row4); return u32x4{x[0], x[1], y[0], y[1]}; };
#pragma unroll
    for (int s = 0; s < 8; ++s) wc[s] = frag(lds + L_WO + (32 * s + 8 * g + q) * 256 + wave * 32 + pp, 4 * 256);
#pragma unroll
    for (int s = 0; s < 7; ++s) wg[s] = frag(lds + L_WG + ((wave >> 1) & 1) * 28672 + ((wave >> 2) * 224 + 32 * s + 8 * g + q) * 64 + (wave & 1) * 32 + pp, 4 * 64);
#pragma unroll
    for (int s = 0; s < 4; ++s) wd[s] = frag(lds + L_WD + (8 * g + q) * (DM * 2) + min(wave + 8 * s, 27) * 32 + pp, 4 * DM * 2);
  }
  STAMP(3);
  const unsigned ib = WS_PART + ((hd >> 1) * 4 + (wave >> 2) * 2 + (hd & 1)) * INBOX;
  unsigned mlo = col < NSPLIT ? ib + col * REC + 1024 : DROP, dof = ib + (wave & 3) * 256 + lane * 4;
  unsigned fo = WS_FLAG + (lane >> 2) * 128 + ((lane & 3) < 3 ? (lane & 3) * 16 : 0);
  if (wave == 0) {
    POLL_BEGIN
    const u32x2 f = LD64(WS_FLAG + 64 + (lane >> 2) * 128 + (lane & 3) * 8);
    POLL_END(((f[0] ^ tlo) | (f[1] ^ thi)) != 0, 8)
    POLL_BEGIN
    const u32x4 f = LD128(fo);
    POLL_END(__builtin_popcountll(__ballot(umax4(f) == EMPTY)) > ARM_LEFT, 16)
  }
  LAS unsigned char* o_wr = lds + L_O + (wave >> 2) * O_ROW + (wave & 3) * 128 + lane * 2;
  LAS unsigned char* o_rd = lds + L_O + (col & 1) * O_ROW + g * 16;
  unsigned y_st = (lane < 32 && wave < ntile) ? yo + g * 1792 + wave * 64 + col * 4 : DROP;
  asm volatile("" : "+v"(mlo), "+v"(dof), "+v"(fo), "+v"(o_wr), "+v"(o_rd), "+v"(y_st));
  BARRIER();
  STAMP(4);
  {
    unsigned ov[NSPLIT];
    u32x2 ml;
    bool fdone = wave != 7;
    POLL_BEGIN
    ml = LD64(mlo);
#pragma unroll
    for (int s = 0; s < NSPLIT; ++s) ov[s] = __builtin_amdgcn_raw_buffer_load_b32(wsr, dof, s * REC, 16);
    if (wave == 7) fdone = !__any(umax4(LD128(fo)) == EMPTY);
    unsigned mx = max(ml[0], ml[1]);
#pragma unroll
    for (int s = 0; s < NSPLIT; s += 2) mx = max(mx, max(ov[s], ov[s + 1]));
    POLL_END(mx == EMPTY, 0)
    STAMP(5);
    const float ms = col < NSPLIT ? bitsf(ml[0]) : -INFINITY;
    float M = fmaxf(ms, dpp<XOR1>(ms));
    M = fmaxf(M, dpp<XOR2>(M)); M = fmaxf(M, dpp<XOR7>(M)); M = fmaxf(M, dpp<ROR8>(M));
    const float a = __builtin_amdgcn_exp2f((ms - M) * C2);
    float L = a * bitsf(ml[1]);
    L += dpp<XOR1>(L); L += dpp<XOR2>(L); L += dpp<XOR7>(L); L += dpp<ROR8>(L);
    float o0 = 0.f, o1 = 0.f;
#pragma unroll
    for (int s = 0; s < NSPLIT; s += 2) {
      o0 = fmaf(bitsf(__builtin_amdgcn_readlane(fbits(a), s)), bitsf(ov[s]), o0);
      o1 = fmaf(bitsf(__builtin_amdgcn_readlane(fbits(a), s + 1)), bitsf(ov[s + 1]), o1);
    }
    const float o = (o0 + o1) * __builtin_amdgcn_rcpf(L);
    *(LAS u16*)o_wr = (u16)cvtpk(o, o);
    if (!fdone) {
      POLL_BEGIN
      const u32x4 f = LD128(fo);
      POLL_END(umax4(f) == EMPTY, 0)
    }
    BARRIER();
    STAMP(6);
    u32x4 af[8];
#pragma unroll
    for (int s = 0; s < 8; ++s) af[s] = *(LAS u32x4*)(o_rd + s * 64);
    f32x4 d0 = {0.f, 0.f, 0.f, 0.f}, d1 = d0;
#pragma unroll
    for (int s = 0; s < 8; s += 2) { d0 = mfma32(af[s], wc[s], d0); d1 = mfma32(af[s + 1], wc[s + 1], d1); }
    ST32(nonempty(fbits((g & 1) ? d0[1] + d1[1] : d0[0] + d1[0])), y_st);
  }
  STAMP(7);
  if (!ffn) { STAMP_OUT(); return; }
  LAS float* red = (LAS float*)(lds + L_RED);
  LAS float* stat = (LAS float*)(lds + L_STAT);
  {
    const unsigned yl = live ? WS_Y + (lane >> 5) * 3584 + (wave >> 2) * 1792 + (wave & 3) * 448 + (lane & 31) * 16 : DROP;
    LAS unsigned char* h_wr = lds + L_XN + (wave >> 2) * XN_ROW + hcol * 2;
    LAS unsigned char* h_rd = lds + ((col & 2) ? L_ONES : L_XN + (col & 1) * XN_ROW + (wave >> 2) * 448) + g * 16;
    __builtin_amdgcn_s_sleep(NAP_Y);
    u32x4 y[8];
    POLL_BEGIN
    unsigned mx = 0;
#pragma unroll
    for (int i = 0; i < 8; ++i) y[i] = __builtin_amdgcn_raw_buffer_load_b128(wsr, yl, i * 7168, 16);
#pragma unroll
    for (int i = 0; i < 8; ++i) mx = max(mx, umax4(y[i]));
    POLL_END(mx == EMPTY, 0)
    STAMP(8);
    f32x4 sa = __builtin_bit_cast(f32x4, y[0]) + __builtin_bit_cast(f32x4, y[1]), sb = __builtin_bit_cast(f32x4, y[2]) + __builtin_bit_cast(f32x4, y[3]);
    sa += __builtin_bit_cast(f32x4, y[4]) + __builtin_bit_cast(f32x4, y[5]);
    sb += __builtin_bit_cast(f32x4, y[6]) + __builtin_bit_cast(f32x4, y[7]);
    sa += sb;
    const auto t0 = __builtin_amdgcn_permlane32_swap(fbits(sa[0]), fbits(sa[2]), false, false);
    const auto t1 = __builtin_amdgcn_permlane32_swap(fbits(sa[1]), fbits(sa[3]), false, false);
    const unsigned at = cvtpk(bitsf(t0[0]) + bitsf(t0[1]), bitsf(t1[0]) + bitsf(t1[1]));
    const unsigned hw = cvtpk(lo16(xw) + lo16(at), hi16(xw) + hi16(at));
    if (live) *(LAS unsigned*)h_wr = hw;
    const float h0 = live ? lo16(hw) : 0.f, h1 = live ? hi16(hw) : 0.f;
    const float mw = swap32_sum(half_sum(h0 + h1)) * (1.f / 112);
    const float e0 = live ? h0 - mw : 0.f, e1_ = live ? h1 - mw : 0.f;
    const float qw = swap32_sum(half_sum(fmaf(e0, e0, e1_ * e1_)));
    if (lane == 0) *(LAS f32x2*)(stat + wave * 2) = f32x2{mw, qw};
    BARRIER();
    STAMP(9);
    if (wave == 7 && sidx == 0) ST64((u32x2{(unsigned)e1, (unsigned)(e1 >> 32)}), lane == 0 ? WS_EPOCH : DROP);
    u32x4 af[7];
#pragma unroll
    for (int s = 0; s < 7; ++s) af[s] = *(LAS u32x4*)(h_rd + s * 64);
    f32x4 d0 = {0.f, 0.f, 0.f, 0.f}, d1 = d0;
#pragma unroll
    for (int s = 0; s < 7; ++s) { if (s & 1) d1 = mfma32(af[s], wg[s], d1); else d0 = mfma32(af[s], wg[s], d0); }
    const f32x4 s01 = *(LAS f32x4*)(stat + (lane >> 5) * 8), s23 = *(LAS f32x4*)(stat + (lane >> 5) * 8 + 4);
    const float mean = ((s01[0] + s01[2]) + (s23[0] + s23[2])) * 0.25f;
    const float c0 = s01[0] - mean, c1 = s01[2] - mean, c2 = s23[0] - mean, c3 = s23[2] - mean;
    const float var = rbf((((s01[1] + s01[3]) + (s23[1] + s23[3])) + 112.f * ((c0 * c0 + c1 * c1) + (c2 * c2 + c3 * c3))) * (1.f / DM));
    const float mb = rbf(mean), rs = __builtin_amdgcn_rcpf(rbf(__builtin_amdgcn_sqrtf(rbf(var + 1e-5f))));
    d0 += d1;
    if (lane < 48) red[wave * 48 + lane] = g == 0 ? d0[0] : (g == 1 ? d0[1] : d0[2]);
    BARRIER();
    STAMP(10);
    const int ro = (g & 1) * 48 + col, rb = (lane >> 5) * 16;
    const float gv = rbf(((red[ro + rb] + red[ro + rb + 192]) - mb * (red[ro + 32] + red[ro + 224])) * rs);
    const float uv = rbf(((red[ro + rb + 96] + red[ro + rb + 288]) - mb * (red[ro + 128] + red[ro + 320])) * rs);
    const float z = (-2.f * 0.7978845608028654f * 1.4426950408889634f) * fmaf(0.044715f * gv * gv, gv, gv);
    const float av = rbf(gv * __builtin_amdgcn_rcpf(1.f + __builtin_amdgcn_exp2f(z))) * uv;
    *(LAS u16*)(lds + L_ACT + wave * 128 + lane * 2) = (u16)cvtpk(av, av);
    const u32x4 aa = *(LAS u32x4*)(lds + L_ACT + wave * 128 + (col & 1) * 64 + g * 16);
    f32x4 dz[4];
#pragma unroll
    for (int s = 0; s < 4; ++s) dz[s] = mfma32(aa, wd[s], f32x4{0.f, 0.f, 0.f, 0.f});
#pragma unroll
    for (int s = 0; s < 4; ++s)
      ST32(nonempty(fbits((g & 1) ? dz[s][1] : dz[s][0])), (lane < 32 && wave + 8 * s < 28) ? WS_Z + ((wave + 8 * s) * 56 + sidx) * 128 + lane * 4 : DROP);
  }
  STAMP(11);
  if (sidx < 28 && wave == 7) {
    const unsigned zl = WS_Z + sidx * 7168 + lane * 16;
    const int oc = ((lane & 3) * 4 + (lane >> 5) + ((lane >> 4) & 1) * 2) + sidx * 16, ob = (lane >> 2) & 1;
    const float h = lo16(*(LAS u16*)(lds + L_XN + ob * XN_ROW + oc * 2));
    __builtin_amdgcn_s_sleep(NAP_Z);
    u32x4 z[7];
    POLL_BEGIN
    unsigned mx = 0;
#pragma unroll
    for (int i = 0; i < 7; ++i) z[i] = __builtin_amdgcn_raw_buffer_load_b128(wsr, zl, i * 1024, 16);
#pragma unroll
    for (int i = 0; i < 7; ++i) mx = max(mx, umax4(z[i]));
    POLL_END(mx == EMPTY, 0)
    STAMP(12);
    f32x4 sa = __builtin_bit_cast(f32x4, z[0]) + __builtin_bit_cast(f32x4, z[1]), sb = __builtin_bit_cast(f32x4, z[2]) + __builtin_bit_cast(f32x4, z[3]);
    sa += __builtin_bit_cast(f32x4, z[4]) + __builtin_bit_cast(f32x4, z[5]);
    sa += sb + __builtin_bit_cast(f32x4, z[6]);
    const auto t0 = __builtin_amdgcn_permlane32_swap(fbits(sa[0]), fbits(sa[1]), false, false);
    const auto t1 = __builtin_amdgcn_permlane32_swap(fbits(sa[2]), fbits(sa[3]), false, false);
    const auto t2 = __builtin_amdgcn_permlane16_swap(fbits(bitsf(t0[0]) + bitsf(t0[1])), fbits(bitsf(t1[0]) + bitsf(t1[1])), false, false);
    float f = bitsf(t2[0]) + bitsf(t2[1]);
    f += dpp<ROR8>(f);
    f = h + rbf(f);
    if (!(lane & 8)) p.out[ob * DM + oc] = (u16)cvtpk(f, f);
    STAMP(13);
  }
  STAMP_OUT();
}

extern "C" void kernel_launch(void* const* d_in, const int* in_sizes, int n_in, void* d_out, int out_size, void* d_ws, size_t ws_size,
                              hipStream_t stream) {
  Params p;
  p.x = (const u16*)d_in[0]; p.kc = (const u16*)d_in[1]; p.vc = (const u16*)d_in[2]; p.wq = (const u16*)d_in[3]; p.wo = (const u16*)d_in[4];
  p.wg = (const u16*)d_in[6]; p.wu = (const u16*)d_in[7]; p.wd = (const u16*)d_in[8];
  p.out = (u16*)d_out; p.ws = (unsigned char*)d_ws;
  fused_decode<<<NWG, 512, 0, stream>>>(p);
}
```

```cpp
#include <hip/hip_runtime.h>
#include <hip/hip_bf16.h>
#include <cstdint>

#define NHOLD 4
#define ORDER 3
#define INFL 2
#define ARM_LEFT 24
#define NAP_Q 24
#define NAP_Y 12
#define NAP_Z 11
#define STAMPS 0

typedef unsigned u32x4 __attribute__((ext_vector_type(4)));
typedef unsigned u32x2 __attribute__((ext_vector_type(2)));
typedef int i32x4 __attribute__((ext_vector_type(4)));
typedef float f32x4 __attribute__((ext_vector_type(4)));
typedef short s16x8 __attribute__((ext_vector_type(8)));
typedef short s16x4 __attribute__((ext_vector_type(4)));
typedef float f32x2 __attribute__((ext_vector_type(2)));
typedef __bf16 bf16x2 __attribute__((ext_vector_type(2)));
typedef unsigned short u16;
typedef unsigned long long u64;
#define LAS __attribute__((address_space(3)))

constexpr int DM = 448, SK = 32768, DH = 256, FF = 1792;
constexpr int NWG = 256, NSERV = 64, NSPLIT = 12;
constexpr int PAIR_TILES = SK / 16;
constexpr unsigned EMPTY = 0xFFFFFFFFu;
constexpr unsigned DROP = 0x40000000u;
constexpr int SPIN_CAP = 1 << 15;

constexpr unsigned REC = 1152, INBOX = NSPLIT * REC;
constexpr unsigned WS_EPOCH = 0, WS_QG = 0x1000, WS_FLAG = 0x11000, WS_Y = 0x12000, WS_Z = 0x20000, WS_PART = 0x60000,
                   WS_BYTES = WS_PART + 32 * INBOX;
constexpr unsigned WS_DBG = 0x100000;
constexpr unsigned SLOT = 8 * 1056, RING = 4 * SLOT, L_Q = 4 * RING, L_REC = L_Q + 1024, RECB = 2304, MRG_ML = 2048, L_SC = L_REC + 4 * RECB, SCB = 2816;
constexpr unsigned WQ_PIECE = 1088, L_WO = 0, L_WG = 65536, L_WD = L_WG + 2 * 28672, L_XN = L_WD + 28672, XN_ROW = 960, L_ONES = L_XN + 2 * XN_ROW,
                   L_RED = L_ONES + 896, L_STAT = L_RED + 1536, LDS_BYTES = L_SC + 4 * SCB;
constexpr unsigned O_ROW = 576, L_O = 0, L_ACT = 2048;
static_assert(LDS_BYTES <= 163840 && L_STAT + 64 <= LDS_BYTES && 56 * WQ_PIECE <= L_XN && WS_Z + 28 * 56 * 128 <= WS_PART, "maps");

struct Params {
  const u16 *x, *kc, *vc, *wq, *wo, *wg, *wu, *wd;
  u16* out;
  unsigned char* ws;
};

__device__ __forceinline__ unsigned cvtpk(float lo, float hi) {
  return __builtin_bit_cast(unsigned, __builtin_convertvector(f32x2{lo, hi}, bf16x2));
}
__device__ __forceinline__ float lo16(unsigned w) { return __builtin_bit_cast(float, w << 16); }
__device__ __forceinline__ float hi16(unsigned w) { return __builtin_bit_cast(float, w & 0xffff0000u); }
__device__ __forceinline__ float rbf(float x) { return lo16(cvtpk(x, x)); }
__device__ __forceinline__ unsigned fbits(float x) { return __builtin_bit_cast(unsigned, x); }
__device__ __forceinline__ float bitsf(unsigned x) { return __builtin_bit_cast(float, x); }
__device__ __forceinline__ unsigned nonempty(unsigned w) { return min(w, EMPTY - 1u); }
__device__ __forceinline__ f32x4 mfma32(u32x4 a, u32x4 b, f32x4 c) {
  return __builtin_amdgcn_mfma_f32_16x16x32_bf16(__builtin_bit_cast(s16x8, a), __builtin_bit_cast(s16x8, b), c, 0, 0, 0);
}
__device__ __forceinline__ f32x4 mfma16(u32x2 a, u32x2 b, f32x4 c) {
  return __builtin_amdgcn_mfma_f32_16x16x16bf16_1k(__builtin_bit_cast(s16x4, a), __builtin_bit_cast(s16x4, b), c, 0, 0, 0);
}
template <int CTRL> __device__ __forceinline__ float dpp(float x) {
  return __builtin_bit_cast(float, __builtin_amdgcn_mov_dpp(__builtin_bit_cast(int, x), CTRL, 0xf, 0xf, true));
}
constexpr int XOR1 = 0xB1, XOR2 = 0x4E, XOR7 = 0x141, ROR8 = 0x128;
__device__ __forceinline__ float swap16_max(float x) { auto s = __builtin_amdgcn_permlane16_swap(fbits(x), fbits(x), false, false); return fmaxf(bitsf(s[0]), bitsf(s[1])); }
__device__ __forceinline__ float swap32_max(float x) { auto s = __builtin_amdgcn_permlane32_swap(fbits(x), fbits(x), false, false); return fmaxf(bitsf(s[0]), bitsf(s[1])); }
__device__ __forceinline__ float swap16_sum(float x) { auto s = __builtin_amdgcn_permlane16_swap(fbits(x), fbits(x), false, false); return bitsf(s[0]) + bitsf(s[1]); }
__device__ __forceinline__ float swap32_sum(float x) { auto s = __builtin_amdgcn_permlane32_swap(fbits(x), fbits(x), false, false); return bitsf(s[0]) + bitsf(s[1]); }
__device__ __forceinline__ float half_sum(float v) {
  v += dpp<XOR1>(v); v += dpp<XOR2>(v); v += dpp<XOR7>(v); v += dpp<ROR8>(v);
  return swap16_sum(v);
}
__device__ __forceinline__ i32x4 make_srd(const void* p, unsigned bytes) {
  const u64 a = (u64)p;
  return i32x4{(int)(unsigned)a, (int)((unsigned)(a >> 32) & 0xffffu), (int)bytes, 0x00020000};
}
#define WAIT_VM(n) asm volatile("s_waitcnt vmcnt(" #n ")" ::: "memory")
#define WAIT_LGKM0() asm volatile("s_waitcnt lgkmcnt(0)" ::: "memory")
#define BARRIER() asm volatile("s_waitcnt lgkmcnt(0)\n\ts_barrier" ::: "memory")

__device__ __forceinline__ void dma_tile(i32x4 srd, unsigned voff, unsigned soff, unsigned lds) {
  unsigned keep;
#define PIECE(o) "s_add_u32 m0, m0, 544\n\ts_nop 0\n\tbuffer_load_dwordx4 %1, %2, %3 offen offset:" #o " nt lds\n\t"
  asm volatile("s_waitcnt lgkmcnt(0)\n\ts_mov_b32 %0, m0\n\ts_mov_b32 m0, %4\n\ts_nop 0\n\tbuffer_load_dwordx4 %1, %2, %3 offen nt lds\n\t"
               PIECE(512) PIECE(1024) PIECE(1536) PIECE(2048) PIECE(2560) PIECE(3072) PIECE(3584) "s_mov_b32 m0, %0"
               : "=&s"(keep) : "v"(voff), "s"(srd), "s"(soff), "s"(lds) : "memory", "scc");
#undef PIECE
}
__device__ __forceinline__ void dma_piece(i32x4 srd, unsigned voff, unsigned soff, unsigned lds) {
  unsigned keep;
  asm volatile("s_mov_b32 %0, m0\n\ts_mov_b32 m0, %4\n\ts_nop 0\n\tbuffer_load_dwordx4 %1, %2, %3 offen nt lds\n\ts_mov_b32 m0, %0"
               : "=&s"(keep) : "v"(voff), "s"(srd), "s"(soff), "s"(lds) : "memory");
}
__device__ __forceinline__ u32x2 tr_read(LAS unsigned char* p) {
  return __builtin_bit_cast(u32x2, __builtin_amdgcn_ds_read_tr16_b64_v4i16((LAS s16x4*)p));
}
__device__ __forceinline__ u32x4 bfrag(LAS unsigned char* p) {
  const u32x2 a = tr_read(p), b = tr_read(p + 128);
  return u32x4{a[0], a[1], b[0], b[1]};
}
__device__ __forceinline__ void layernorm16(const float (&x)[16], bool live, u32x4& y0, u32x4& y1) {
  float s = 0.f;
#pragma unroll
  for (int i = 0; i < 16; ++i) s += x[i];
  const float mean = half_sum(live ? s : 0.f) * (1.f / DM);
  float v = 0.f;
#pragma unroll
  for (int i = 0; i < 16; ++i) { const float d = x[i] - mean; v = fmaf(d, d, v); }
  const float var = rbf(half_sum(live ? v : 0.f) * (1.f / DM));
  const float mb = rbf(mean), rs = 1.f / rbf(sqrtf(rbf(var + 1e-5f)));
  float y[16];
#pragma unroll
  for (int i = 0; i < 16; i += 2) { const unsigned w = cvtpk(x[i] - mb, x[i + 1] - mb); y[i] = lo16(w) * rs; y[i + 1] = hi16(w) * rs; }
  y0 = u32x4{cvtpk(y[0], y[1]), cvtpk(y[2], y[3]), cvtpk(y[4], y[5]), cvtpk(y[6], y[7])};
  y1 = u32x4{cvtpk(y[8], y[9]), cvtpk(y[10], y[11]), cvtpk(y[12], y[13]), cvtpk(y[14], y[15])};
}
__device__ __forceinline__ unsigned umax4(u32x4 v) { return max(max(v[0], v[1]), max(v[2], v[3])); }

#define LD128(off) __builtin_amdgcn_raw_buffer_load_b128(wsr, (off), 0, 16)
#define LD64(off) __builtin_amdgcn_raw_buffer_load_b64(wsr, (off), 0, 16)
#define LD32(off) __builtin_amdgcn_raw_buffer_load_b32(wsr, (off), 0, 16)
#define ST128(v, off) __builtin_amdgcn_raw_buffer_store_b128((v), wsr, (off), 0, 16)
#define ST64(v, off) __builtin_amdgcn_raw_buffer_store_b64((v), wsr, (off), 0, 16)
#define ST32(v, off) __builtin_amdgcn_raw_buffer_store_b32((v), wsr, (off), 0, 16)
#define POLL_BEGIN for (int spin_ = SPIN_CAP;;) { asm volatile("" ::: "memory");
#define POLL_END(pending, nap) if (!__any(pending) || --spin_ <= 0) break; if (nap) __builtin_amdgcn_s_sleep(nap); }
#define STAMP(i)
#define STAMP_OUT()
#define TL(i)
#define TL_OUT()

extern "C" __global__ void __launch_bounds__(512, 2) fused_decode(Params p) {
  __shared__ __attribute__((aligned(16))) unsigned char smem[LDS_BYTES];
  LAS unsigned char* lds = (LAS unsigned char*)smem;
  const unsigned lds0 = (unsigned)(size_t)lds;
  const int bid = blockIdx.x, lane = threadIdx.x & 63, wave = __builtin_amdgcn_readfirstlane(threadIdx.x >> 6);
  const int g = lane >> 4, col = lane & 15;
  const int xcd = bid & 7, idx = bid >> 3;
  const auto wsr = __builtin_amdgcn_make_buffer_rsrc(p.ws, 0, WS_BYTES, 0x00020000);
  constexpr float C2 = 0.0625f * 1.4426950408889634f;
  STAMP(0);

  if (idx >= NSERV / 8) {
    const int j = (idx - NSERV / 8) * 8 + xcd, pair = j / NSPLIT, mem = j % NSPLIT;
    if (wave > 4) return;
    if (wave == 0) {
      const u32x2 ep = LD64(WS_EPOCH);
      const u64 e1 = (((u64)ep[1] << 32) | ep[0]) + 1;
      const unsigned tlo = (unsigned)e1 << 16, thi = (unsigned)(e1 >> 16);
      STAMP(1);
      __builtin_amdgcn_s_sleep(NAP_Q);
      u32x4 qg[4];
      POLL_BEGIN
      unsigned bad = 0;
#pragma unroll
      for (int i = 0; i < 4; ++i) qg[i] = LD128(WS_QG + (pair >> 3) * 32768 + (pair & 7) * 4096 + i * 1024 + lane * 16);
#pragma unroll
      for (int i = 0; i < 4; ++i) bad |= ((qg[i][0] ^ tlo) >> 16) | (qg[i][1] ^ thi) | ((qg[i][2] ^ tlo) >> 16) | (qg[i][3] ^ thi);
      POLL_END(bad != 0, 0)
#pragma unroll
      for (int i = 0; i < 4; ++i) *(LAS unsigned*)(lds + L_Q + i * 256 + lane * 4) = (qg[i][0] & 0xffffu) | (qg[i][2] << 16);
      BARRIER();
      STAMP(2);
      STAMP_OUT();
      return;
    }
    const i32x4 ksrd = make_srd(p.kc + (size_t)pair * SK * DH, SK * DH * 2), vsrd = make_srd(p.vc + (size_t)pair * SK * DH, SK * DH * 2);
    const unsigned voff = (lane >> 5) * 4096 + (lane & 31) * 16;
    const int sw = wave - 1;
    const unsigned ring = sw * RING;
    const int wid = mem * 4 + sw;
    const int n = (PAIR_TILES - wid + 4 * NSPLIT - 1) / (4 * NSPLIT);
    const unsigned so0 = wid * 8192u, sstep = 4 * NSPLIT * 8192u;
    auto issue = [&](int u) {
      if (u < n) dma_tile(ksrd, voff, so0 + u * sstep, lds0 + ring + (u & 3) * SLOT);
      else dma_tile(vsrd, voff, so0 + (u - n) * sstep, lds0 + ring + (u & 3) * SLOT);
    };
    LAS unsigned char* krd = lds + ring + (col & 7) * 1056 + (col >> 3) * 512 + g * 16;
    const int vrow = 4 * g + (col >> 2);
    LAS unsigned char* vrd = lds + ring + (vrow & 7) * 1056 + (vrow >> 3) * 512 + (col & 3) * 8;
    LAS unsigned char* scp = lds + L_SC + sw * SCB + (col & 1) * 32 + g * 8;
    u32x4 hk[NHOLD][8];
    issue(0); issue(1);
#pragma unroll
    for (int u = 0; u < NHOLD; ++u) {
      WAIT_VM(8); TL(u); issue(u + 2);
#pragma unroll
      for (int s = 0; s < 8; ++s) hk[u][s] = *(LAS u32x4*)(krd + (u & 3) * SLOT + s * 64);
    }
    WAIT_VM(8); TL(NHOLD); issue(NHOLD + 2);
    WAIT_VM(8); TL(NHOLD + 1); issue(NHOLD + 3);
    BARRIER();
    STAMP(1);
    u32x4 qf[8];
#pragma unroll
    for (int s = 0; s < 8; ++s) qf[s] = *(LAS u32x4*)(lds + L_Q + (col & 1) * 512 + s * 64 + g * 16);
    float m = -INFINITY;
    auto scores = [&](int u, auto kf) {
      f32x4 d0 = {0.f, 0.f, 0.f, 0.f}, d1 = d0;
#pragma unroll
      for (int s = 0; s < 8; s += 2) { d0 = mfma32(kf(s), qf[s], d0); d1 = mfma32(kf(s + 1), qf[s + 1], d1); }
      const unsigned w01 = cvtpk(d0[0] + d1[0], d0[1] + d1[1]), w23 = cvtpk(d0[2] + d1[2], d0[3] + d1[3]);
      m = fmaxf(fmaxf(m, fmaxf(lo16(w01), hi16(w01))), fmaxf(lo16(w23), hi16(w23)));
      if (col < 2) *(LAS u32x2*)(scp + u * 64) = u32x2{w01, w23};
    };
    auto ring_scores = [&](int u) { scores(u, [&](int s) { return *(LAS u32x4*)(krd + (u & 3) * SLOT + s * 64); }); };
    ring_scores(NHOLD); issue(NHOLD + 4);
    ring_scores(NHOLD + 1); issue(NHOLD + 5);
#pragma unroll
    for (int u = 0; u < NHOLD; ++u) scores(u, [&](int s) { return hk[u][s]; });
    WAIT_VM(24); TL(NHOLD + 2); ring_scores(NHOLD + 2);
    WAIT_VM(16); TL(NHOLD + 3); ring_scores(NHOLD + 3);
    constexpr int U0 = NHOLD + 4;
#define WAIT_STEP() WAIT_VM(8)
    STAMP(5);
#pragma unroll 1
    for (int u = U0; u < n; ++u) { WAIT_STEP(); TL(u); issue(u + INFL); ring_scores(u); }
    m = swap32_max(swap16_max(m));
    const float m2 = fmaxf(m, -3.0e38f) * C2;
    unsigned pub_o = WS_PART + (((pair & 7) * 2 + (pair >> 3)) * 2 + sw) * INBOX + mem * REC + lane * 16;
    unsigned pub_ml = lane == 0 ? pub_o + 1024 : DROP;
    LAS unsigned char* mrd_o = lds + L_REC + (lane >> 2) * 128 + sw * 64 + (lane & 3) * 16;
    LAS unsigned char* mrd_ml = lds + L_REC + MRG_ML + sw * 8;
    asm volatile("" : "+v"(pub_o), "+v"(pub_ml), "+v"(mrd_o), "+v"(mrd_ml));
    f32x4 acc[16];
#pragma unroll
    for (int k = 0; k < 16; ++k) acc[k] = f32x4{0.f, 0.f, 0.f, 0.f};
    float l = 0.f;
#pragma unroll 1
    for (int v = 0; v < n; ++v) {
      const u32x2 sw2 = *(LAS u32x2*)(scp + v * 64);
      const float p0 = __builtin_amdgcn_exp2f(fmaf(lo16(sw2[0]), C2, -m2)), p1 = __builtin_amdgcn_exp2f(fmaf(hi16(sw2[0]), C2, -m2));
      const float p2 = __builtin_amdgcn_exp2f(fmaf(lo16(sw2[1]), C2, -m2)), p3 = __builtin_amdgcn_exp2f(fmaf(hi16(sw2[1]), C2, -m2));
      l += (p0 + p1) + (p2 + p3);
      u32x2 pf = u32x2{cvtpk(p0, p1), cvtpk(p2, p3)};
      asm volatile("" : "+v"(pf), "+v"(l));
      if (v + INFL < n) { WAIT_STEP(); issue(n + v + INFL); }
      else if (v + 2 < n) WAIT_VM(16);
      else if (v + 1 < n) WAIT_VM(8);
      else WAIT_VM(0);
      TL(n + v);
      LAS unsigned char* vp = vrd + ((n + v) & 3) * SLOT;
#pragma unroll
      for (int k = 0; k < 16; ++k) acc[k] = mfma16(pf, tr_read(vp + k * 32), acc[k]);
    }
    STAMP(2);
    l = swap32_sum(swap16_sum(l));
    LAS float* rec = (LAS float*)(lds + L_REC + sw * RECB);
    if (lane < 16) {
#pragma unroll
      for (int k = 0; k < 16; ++k) { rec[k * 32 + lane] = acc[k][0]; rec[k * 32 + 16 + lane] = acc[k][1]; }
      if (lane < 2) { rec[MRG_ML / 4 + 2 * lane] = m; rec[MRG_ML / 4 + 2 * lane + 1] = l; }
    }
    BARRIER();
    STAMP(3);
    if (sw < 2) {
      f32x2 ml[4];
      f32x4 ow[4];
#pragma unroll
      for (int k = 0; k < 4; ++k) { ml[k] = *(LAS f32x2*)(mrd_ml + k * RECB); ow[k] = *(LAS f32x4*)(mrd_o + k * RECB); }
      const float M = fmaxf(fmaxf(fmaxf(ml[0][0], ml[1][0]), fmaxf(ml[2][0], ml[3][0])), -3.0e38f);
      float L = 0.f;
      f32x4 o = {0.f, 0.f, 0.f, 0.f};
#pragma unroll
      for (int k = 0; k < 4; ++k) { const float a = __builtin_amdgcn_exp2f((ml[k][0] - M) * C2); L = fmaf(a, ml[k][1], L); o += a * ow[k]; }
      ST128((u32x4{nonempty(fbits(o[0])), nonempty(fbits(o[1])), nonempty(fbits(o[2])), nonempty(fbits(o[3]))}), pub_o);
      ST64((u32x2{nonempty(fbits(M)), nonempty(fbits(L))}), pub_ml);
    } else if (sw == 2) {
      ST32(0u, lane == 0 ? WS_FLAG + pair * 128 + mem * 4 : DROP);
    }
    STAMP(4);
    STAMP_OUT();
    TL_OUT();
    return;
  }

  const int sidx = xcd * 8 + idx, hd = sidx >> 2, cq = sidx & 3;
  const bool ffn = sidx < 56;
  const bool live = (lane & 31) < 28;
  const u32x4 ee = {EMPTY, EMPTY, EMPTY, EMPTY};
  u32x4 xa, xb;
  if (wave == 7) {
    const int xo = (lane >> 5) * DM + (live ? (lane & 31) : 0) * 16;
    xa = *(const u32x4*)(p.x + xo); xb = *(const u32x4*)(p.x + xo + 8);
  } else {
    const i32x4 qsrd = make_srd(p.wq, DM * 4096 * 2);
    const unsigned qv = (lane >> 3) * 8192 + (lane & 7) * 16 + sidx * 128;
#pragma unroll
    for (int s = 0; s < 8; ++s) dma_piece(qsrd, qv, (wave * 8 + s) * 8 * 8192, lds0 + (wave * 8 + s) * WQ_PIECE);
  }
  {
    unsigned co = wave * 1024 + lane * 16 < INBOX / 2 ? WS_PART + sidx * (INBOX / 2) + wave * 1024 + lane * 16 : DROP;
    if (wave == 7 && (sidx & 3) == 0) {
      const int pr = ((sidx >> 2) & 1) * 8 + (sidx >> 3);
      if (lane < 4) co = WS_FLAG + pr * 128 + lane * 16;
    }
    ST128(ee, co);
  }
  const u32x2 ep = LD64(WS_EPOCH);
  if (wave == 7) {
    float xf[16];
#pragma unroll
    for (int k = 0; k < 4; ++k) { xf[2 * k] = lo16(xa[k]); xf[2 * k + 1] = hi16(xa[k]); xf[8 + 2 * k] = lo16(xb[k]); xf[9 + 2 * k] = hi16(xb[k]); }
    u32x4 y0, y1;
    layernorm16(xf, live, y0, y1);
    LAS unsigned char* xn = lds + L_XN + (lane >> 5) * XN_ROW + (lane & 31) * 32;
    if (live) { *(LAS u32x4*)xn = y0; *(LAS u32x4*)(xn + 16) = y1; }
    if (lane < 56) *(LAS u32x4*)(lds + L_ONES + lane * 16) = u32x4{0x3F803F80u, 0x3F803F80u, 0x3F803F80u, 0x3F803F80u};
  }
  WAIT_VM(0);
  STAMP(1);
  BARRIER();
  const u64 e1 = (((u64)ep[1] << 32) | ep[0]) + 1;
  const unsigned tlo = (unsigned)e1 << 16, thi = (unsigned)(e1 >> 16);
  if (wave < 4) {
    f32x4 d0 = {0.f, 0.f, 0.f, 0.f}, d1 = d0;
    LAS unsigned char* ap = lds + L_XN + (col & 1) * XN_ROW + g * 16;
    LAS unsigned char* bp = lds + g * WQ_PIECE + (col >> 2) * 128 + wave * 32 + (col & 3) * 8;
#pragma unroll
    for (int s = 0; s < 14; s += 2) {
      const u32x2 b0 = tr_read(bp + s * 4 * WQ_PIECE), b1 = tr_read(bp + s * 4 * WQ_PIECE + 512);
      const u32x2 b2 = tr_read(bp + (s + 1) * 4 * WQ_PIECE), b3 = tr_read(bp + (s + 1) * 4 * WQ_PIECE + 512);
      d0 = mfma32(*(LAS u32x4*)(ap + s * 64), u32x4{b0[0], b0[1], b1[0], b1[1]}, d0);
      d1 = mfma32(*(LAS u32x4*)(ap + s * 64 + 64), u32x4{b2[0], b2[1], b3[0], b3[1]}, d1);
    }
    const float qv = (g & 1) ? d0[1] + d1[1] : d0[0] + d1[0];
    ST64((u32x2{tlo | (cvtpk(qv, qv) & 0xffffu), thi}), lane < 32 ? WS_QG + g * 32768 + (sidx * 64 + wave * 16 + col) * 8 : DROP);
  }
  STAMP(2);
  BARRIER();
  const int ntile = cq < 3 ? 8 : 4;
  const unsigned yo = WS_Y + hd * 3584 + cq * 512;
  ST128(ee, lane < 2 * ntile * 4 && wave == 0 ? yo + (lane / (ntile * 4)) * 1792 + (lane % (ntile * 4)) * 16 : DROP);
  if (ffn && wave < 4) ST128(ee, wave * 8 + (lane >> 3) < 28 ? WS_Z + ((wave * 8 + (lane >> 3)) * 56 + sidx) * 128 + (lane & 7) * 16 : DROP);
  {
    const i32x4 osrd = make_srd(p.wo, 4096 * DM * 2), gsrd = make_srd(wave < 4 ? p.wg : p.wu, DM * FF * 2), dsrd = make_srd(p.wd, FF * DM * 2);
    const unsigned ov = (lane & 15) < ntile * 2 ? (lane >> 4) * (DM * 2) + (lane & 15) * 16 : DROP;
#pragma unroll
    for (int s = 0; s < 8; ++s) dma_piece(osrd, ov, (hd * 256 + (wave * 8 + s) * 4) * (DM * 2) + cq * 256, lds0 + L_WO + (wave * 8 + s) * 1024);
    if (ffn) {
#pragma unroll
      for (int s = 0; s < 7; ++s) dma_piece(gsrd, (lane >> 2) * (FF * 2) + (lane & 3) * 16, ((wave & 3) * 7 + s) * 16 * (FF * 2) + sidx * 64, lds0 + L_WG + (wave * 7 + s) * 1024);
      if (wave < 7) {
#pragma unroll
        for (int s = 0; s < 4; ++s) dma_piece(dsrd, lane * 16, sidx * 28672 + (wave * 4 + s) * 1024, lds0 + L_WD + (wave * 4 + s) * 1024);
      }
    }
  }
  const int hcol = (wave & 3) * 112 + (live ? (lane & 31) : 0) * 4 + (lane >> 5) * 2;
  const unsigned xw = *(const unsigned*)(p.x + (wave >> 2) * DM + hcol);
  WAIT_VM(0);
  BARRIER();
  if (wave == 0) ST64((u32x2{tlo, thi}), lane == 0 ? WS_FLAG + 64 + (sidx >> 2) * 128 + (sidx & 3) * 8 : DROP);
  u32x4 wc[8], wg[7], wd[4];
  {
    const int q = col >> 2, pp = (col & 3) * 8;
    auto frag = [&](LAS unsigned char* a, int row4) { const u32x2 x = tr_read(a), y = tr_read(a + row4); return u32x4{x[0], x[1], y[0], y[1]}; };
#pragma unroll
    for (int s = 0; s < 8; ++s) wc[s] = frag(lds + L_WO + (32 * s + 8 * g + q) * 256 + wave * 32 + pp, 4 * 256);
#pragma unroll
    for (int s = 0; s < 7; ++s) wg[s] = frag(lds + L_WG + ((wave >> 1) & 1) * 28672 + ((wave >> 2) * 224 + 32 * s + 8 * g + q) * 64 + (wave & 1) * 32 + pp, 4 * 64);
#pragma unroll
    for (int s = 0; s < 4; ++s) wd[s] = frag(lds + L_WD + (8 * g + q) * (DM * 2) + min(wave + 8 * s, 27) * 32 + pp, 4 * DM * 2);
  }
  STAMP(3);
  const unsigned ib = WS_PART + ((hd >> 1) * 4 + (wave >> 2) * 2 + (hd & 1)) * INBOX;
  unsigned mlo = col < NSPLIT ? ib + col * REC + 1024 : DROP, dof = ib + (wave & 3) * 256 + lane * 4;
  unsigned fo = WS_FLAG + (lane >> 2) * 128 + ((lane & 3) < 3 ? (lane & 3) * 16 : 0);
  if (wave == 0) {
    POLL_BEGIN
    const u32x2 f = LD64(WS_FLAG + 64 + (lane >> 2) * 128 + (lane & 3) * 8);
    POLL_END(((f[0] ^ tlo) | (f[1] ^ thi)) != 0, 8)
    POLL_BEGIN
    const u32x4 f = LD128(fo);
    POLL_END(__builtin_popcountll(__ballot(umax4(f) == EMPTY)) > ARM_LEFT, 16)
  }
  LAS unsigned char* o_wr = lds + L_O + (wave >> 2) * O_ROW + (wave & 3) * 128 + lane * 2;
  LAS unsigned char* o_rd = lds + L_O + (col & 1) * O_ROW + g * 16;
  unsigned y_st = (lane < 32 && wave < ntile) ? yo + g * 1792 + wave * 64 + col * 4 : DROP;
  asm volatile("" : "+v"(mlo), "+v"(dof), "+v"(fo), "+v"(o_wr), "+v"(o_rd), "+v"(y_st));
  BARRIER();
  STAMP(4);
  {
    unsigned ov[NSPLIT];
    u32x2 ml;
    bool fdone = wave != 7;
    POLL_BEGIN
    ml = LD64(mlo);
#pragma unroll
    for (int s = 0; s < NSPLIT; ++s) ov[s] = __builtin_amdgcn_raw_buffer_load_b32(wsr, dof, s * REC, 16);
    if (wave == 7) fdone = !__any(umax4(LD128(fo)) == EMPTY);
    unsigned mx = max(ml[0], ml[1]);
#pragma unroll
    for (int s = 0; s < NSPLIT; s += 2) mx = max(mx, max(ov[s], ov[s + 1]));
    POLL_END(mx == EMPTY, 0)
    STAMP(5);
    const float ms = col < NSPLIT ? bitsf(ml[0]) : -INFINITY;
    float M = fmaxf(ms, dpp<XOR1>(ms));
    M = fmaxf(M, dpp<XOR2>(M)); M = fmaxf(M, dpp<XOR7>(M)); M = fmaxf(M, dpp<ROR8>(M));
    const float a = __builtin_amdgcn_exp2f((ms - M) * C2);
    float L = a * bitsf(ml[1]);
    L += dpp<XOR1>(L); L += dpp<XOR2>(L); L += dpp<XOR7>(L); L += dpp<ROR8>(L);
    float o0 = 0.f, o1 = 0.f;
#pragma unroll
    for (int s = 0; s < NSPLIT; s += 2) {
      o0 = fmaf(bitsf(__builtin_amdgcn_readlane(fbits(a), s)), bitsf(ov[s]), o0);
      o1 = fmaf(bitsf(__builtin_amdgcn_readlane(fbits(a), s + 1)), bitsf(ov[s + 1]), o1);
    }
    const float o = (o0 + o1) * __builtin_amdgcn_rcpf(L);
    *(LAS u16*)o_wr = (u16)cvtpk(o, o);
    if (!fdone) {
      POLL_BEGIN
      const u32x4 f = LD128(fo);
      POLL_END(umax4(f) == EMPTY, 0)
    }
    BARRIER();
    STAMP(6);
    u32x4 af[8];
#pragma unroll
    for (int s = 0; s < 8; ++s) af[s] = *(LAS u32x4*)(o_rd + s * 64);
    f32x4 d0 = {0.f, 0.f, 0.f, 0.f}, d1 = d0;
#pragma unroll
    for (int s = 0; s < 8; s += 2) { d0 = mfma32(af[s], wc[s], d0); d1 = mfma32(af[s + 1], wc[s + 1], d1); }
    ST32(nonempty(fbits((g & 1) ? d0[1] + d1[1] : d0[0] + d1[0])), y_st);
  }
  STAMP(7);
  if (!ffn) { STAMP_OUT(); return; }
  LAS float* red = (LAS float*)(lds + L_RED);
  LAS float* stat = (LAS float*)(lds + L_STAT);
  {
    const unsigned yl = live ? WS_Y + (lane >> 5) * 3584 + (wave >> 2) * 1792 + (wave & 3) * 448 + (lane & 31) * 16 : DROP;
    LAS unsigned char* h_wr = lds + L_XN + (wave >> 2) * XN_ROW + hcol * 2;
    LAS unsigned char* h_rd = lds + ((col & 2) ? L_ONES : L_XN + (col & 1) * XN_ROW + (wave >> 2) * 448) + g * 16;
    __builtin_amdgcn_s_sleep(NAP_Y);
    u32x4 y[8];
    POLL_BEGIN
    unsigned mx = 0;
#pragma unroll
    for (int i = 0; i < 8; ++i) y[i] = __builtin_amdgcn_raw_buffer_load_b128(wsr, yl, i * 7168, 16);
#pragma unroll
    for (int i = 0; i < 8; ++i) mx = max(mx, umax4(y[i]));
    POLL_END(mx == EMPTY, 0)
    STAMP(8);
    f32x4 sa = __builtin_bit_cast(f32x4, y[0]) + __builtin_bit_cast(f32x4, y[1]), sb = __builtin_bit_cast(f32x4, y[2]) + __builtin_bit_cast(f32x4, y[3]);
    sa += __builtin_bit_cast(f32x4, y[4]) + __builtin_bit_cast(f32x4, y[5]);
    sb += __builtin_bit_cast(f32x4, y[6]) + __builtin_bit_cast(f32x4, y[7]);
    sa += sb;
    const auto t0 = __builtin_amdgcn_permlane32_swap(fbits(sa[0]), fbits(sa[2]), false, false);
    const auto t1 = __builtin_amdgcn_permlane32_swap(fbits(sa[1]), fbits(sa[3]), false, false);
    const unsigned at = cvtpk(bitsf(t0[0]) + bitsf(t0[1]), bitsf(t1[0]) + bitsf(t1[1]));
    const unsigned hw = cvtpk(lo16(xw) + lo16(at), hi16(xw) + hi16(at));
    if (live) *(LAS unsigned*)h_wr = hw;
    const float h0 = live ? lo16(hw) : 0.f, h1 = live ? hi16(hw) : 0.f;
    const float mw = swap32_sum(half_sum(h0 + h1)) * (1.f / 112);
    const float e0 = live ? h0 - mw : 0.f, e1_ = live ? h1 - mw : 0.f;
    const float qw = swap32_sum(half_sum(fmaf(e0, e0, e1_ * e1_)));
    if (lane == 0) *(LAS f32x2*)(stat + wave * 2) = f32x2{mw, qw};
    BARRIER();
    STAMP(9);
    if (wave == 7 && sidx == 0) ST64((u32x2{(unsigned)e1, (unsigned)(e1 >> 32)}), lane == 0 ? WS_EPOCH : DROP);
    u32x4 af[7];
#pragma unroll
    for (int s = 0; s < 7; ++s) af[s] = *(LAS u32x4*)(h_rd + s * 64);
    f32x4 d0 = {0.f, 0.f, 0.f, 0.f}, d1 = d0;
#pragma unroll
    for (int s = 0; s < 7; ++s) { if (s & 1) d1 = mfma32(af[s], wg[s], d1); else d0 = mfma32(af[s], wg[s], d0); }
    const f32x4 s01 = *(LAS f32x4*)(stat + (lane >> 5) * 8), s23 = *(LAS f32x4*)(stat + (lane >> 5) * 8 + 4);
    const float mean = ((s01[0] + s01[2]) + (s23[0] + s23[2])) * 0.25f;
    const float c0 = s01[0] - mean, c1 = s01[2] - mean, c2 = s23[0] - mean, c3 = s23[2] - mean;
    const float var = rbf((((s01[1] + s01[3]) + (s23[1] + s23[3])) + 112.f * ((c0 * c0 + c1 * c1) + (c2 * c2 + c3 * c3))) * (1.f / DM));
    const float mb = rbf(mean), rs = __builtin_amdgcn_rcpf(rbf(__builtin_amdgcn_sqrtf(rbf(var + 1e-5f))));
    d0 += d1;
    if (lane < 48) red[wave * 48 + lane] = g == 0 ? d0[0] : (g == 1 ? d0[1] : d0[2]);
    BARRIER();
    STAMP(10);
    const int ro = (g & 1) * 48 + col, rb = (lane >> 5) * 16;
    const float gv = rbf(((red[ro + rb] + red[ro + rb + 192]) - mb * (red[ro + 32] + red[ro + 224])) * rs);
    const float uv = rbf(((red[ro + rb + 96] + red[ro + rb + 288]) - mb * (red[ro + 128] + red[ro + 320])) * rs);
    const float z = (-2.f * 0.7978845608028654f * 1.4426950408889634f) * fmaf(0.044715f * gv * gv, gv, gv);
    const float av = rbf(gv * __builtin_amdgcn_rcpf(1.f + __builtin_amdgcn_exp2f(z))) * uv;
    *(LAS u16*)(lds + L_ACT + wave * 128 + lane * 2) = (u16)cvtpk(av, av);
    const u32x4 aa = *(LAS u32x4*)(lds + L_ACT + wave * 128 + (col & 1) * 64 + g * 16);
    f32x4 dz[4];
#pragma unroll
    for (int s = 0; s < 4; ++s) dz[s] = mfma32(aa, wd[s], f32x4{0.f, 0.f, 0.f, 0.f});
#pragma unroll
    for (int s = 0; s < 4; ++s)
      ST32(nonempty(fbits((g & 1) ? dz[s][1] : dz[s][0])), (lane < 32 && wave + 8 * s < 28) ? WS_Z + ((wave + 8 * s) * 56 + sidx) * 128 + lane * 4 : DROP);
  }
  STAMP(11);
  if (sidx < 28 && wave == 7) {
    const unsigned zl = WS_Z + sidx * 7168 + lane * 16;
    const int oc = ((lane & 3) * 4 + (lane >> 5) + ((lane >> 4) & 1) * 2) + sidx * 16, ob = (lane >> 2) & 1;
    const float h = lo16(*(LAS u16*)(lds + L_XN + ob * XN_ROW + oc * 2));
    __builtin_amdgcn_s_sleep(NAP_Z);
    u32x4 z[7];
    POLL_BEGIN
    unsigned mx = 0;
#pragma unroll
    for (int i = 0; i < 7; ++i) z[i] = __builtin_amdgcn_raw_buffer_load_b128(wsr, zl, i * 1024, 16);
#pragma unroll
    for (int i = 0; i < 7; ++i) mx = max(mx, umax4(z[i]));
    POLL_END(mx == EMPTY, 0)
    STAMP(12);
    f32x4 sa = __builtin_bit_cast(f32x4, z[0]) + __builtin_bit_cast(f32x4, z[1]), sb = __builtin_bit_cast(f32x4, z[2]) + __builtin_bit_cast(f32x4, z[3]);
    sa += __builtin_bit_cast(f32x4, z[4]) + __builtin_bit_cast(f32x4, z[5]);
    sa += sb + __builtin_bit_cast(f32x4, z[6]);
    const auto t0 = __builtin_amdgcn_permlane32_swap(fbits(sa[0]), fbits(sa[1]), false, false);
    const auto t1 = __builtin_amdgcn_permlane32_swap(fbits(sa[2]), fbits(sa[3]), false, false);
    const auto t2 = __builtin_amdgcn_permlane16_swap(fbits(bitsf(t0[0]) + bitsf(t0[1])), fbits(bitsf(t1[0]) + bitsf(t1[1])), false, false);
    float f = bitsf(t2[0]) + bitsf(t2[1]);
    f += dpp<ROR8>(f);
    f = h + rbf(f);
    if (!(lane & 8)) p.out[ob * DM + oc] = (u16)cvtpk(f, f);
    STAMP(13);
  }
  STAMP_OUT();
}

extern "C" void kernel_launch(void* const* d_in, const int* in_sizes, int n_in, void* d_out, int out_size, void* d_ws, size_t ws_size,
                              hipStream_t stream) {
  Params p;
  p.x = (const u16*)d_in[0]; p.kc = (const u16*)d_in[1]; p.vc = (const u16*)d_in[2]; p.wq = (const u16*)d_in[3]; p.wo = (const u16*)d_in[4];
  p.wg = (const u16*)d_in[6]; p.wu = (const u16*)d_in[7]; p.wd = (const u16*)d_in[8];
  p.out = (u16*)d_out; p.ws = (unsigned char*)d_ws;
  fused_decode<<<NWG, 512, 0, stream>>>(p);
}
```
